```python
import torch

def _softmax(x, axis=-1):
    return torch.softmax(x, dim=axis)
def _gelu(x):
    return torch.nn.functional.gelu(x, approximate="tanh")
def _silu(x):
    return torch.nn.functional.silu(x)
def _sigmoid(x):
    return torch.sigmoid(x)
def _layernorm(x, axis=-1, eps=1e-5):
    m = x.mean(dim=axis, keepdim=True)
    v = x.var(dim=axis, keepdim=True, unbiased=False)
    return (x - m) / torch.sqrt(v + eps)
def _scatter_add(tgt, idx, src, axis=0):
    return tgt.clone().index_add_(axis, idx, src)
def _segment_sum(src, seg, n):
    out = torch.zeros((n,) + src.shape[1:], dtype=src.dtype)
    return out.index_add_(0, seg, src)
def _pad(x, pw):
    flat = tuple(v for pair in reversed(pw) for v in pair)
    return torch.nn.functional.pad(x, flat)

kernel_name = 'parallel_sum__mha_block_cached__glu_gated_bilinear__decode'

def setup_inputs():

    _w = (lambda a: (lambda t: t.to(torch.bfloat16) if t.is_floating_point() else t)(torch.as_tensor(a)))
    def _A_setup():
        B, Sq, D = 4, 1, 640
        H, Sk, Dh = 32, 4096, 128
        return {
            "x": _w((lambda *s: torch.randn(*s, dtype=torch.bfloat16) * 0.05)(B, Sq, D)),
            "k_cache": _w((lambda *s: torch.randn(*s, dtype=torch.bfloat16) * 1.6)(B, H, Sk, Dh)),
            "v_cache": _w((lambda *s: torch.randn(*s, dtype=torch.bfloat16) * 0.8)(B, H, Sk, Dh)),
            "wq": _w((lambda *s: torch.randn(*s, dtype=torch.bfloat16) * 1.6)(D, H * Dh) * (D ** -0.5)),
            "wo": _w((lambda *s: torch.randn(*s, dtype=torch.bfloat16) * 0.8)(H * Dh, D) * ((H * Dh) ** -0.5)),
        }
    _A_in = _A_setup()

    def _B_setup():
        B, S, D, F = 4, 1, 640, 1728
        return {
            "x": _w((lambda *s: torch.randn(*s, dtype=torch.bfloat16) * 0.05)(B, S, D)),
            "w_gate": _w((lambda *s: torch.randn(*s, dtype=torch.bfloat16) * 0.4)(D, F) * (D ** -0.5)),
            "w_up": _w((lambda *s: torch.randn(*s, dtype=torch.bfloat16) * 0.4)(D, F) * (D ** -0.5)),
            "w_down": _w((lambda *s: torch.randn(*s, dtype=torch.bfloat16) * 0.4)(F, D) * (F ** -0.5)),
        }
    _B_in = _B_setup()

    return {
        "A_x": _A_in["x"],
        "A_k_cache": _A_in["k_cache"],
        "A_v_cache": _A_in["v_cache"],
        "A_wq": _A_in["wq"],
        "A_wo": _A_in["wo"],
        "B_x": _B_in["x"],
        "B_w_gate": _B_in["w_gate"],
        "B_w_up": _B_in["w_up"],
        "B_w_down": _B_in["w_down"],
    }

def reference(A_x, A_k_cache, A_v_cache, A_wq, A_wo, B_x, B_w_gate, B_w_up, B_w_down):
    def _A_ref(x, k_cache, v_cache, wq, wo):
        B, Sq, D = x.shape
        H, Dh = 32, 128
        q = (lambda x, *d: x.permute(*d))((x @ wq).reshape(B, Sq, H, Dh), 0, 2, 1, 3)
        scores = torch.einsum("bhqd,bhkd->bhqk", q, k_cache) * (Dh ** -0.5)
        p = _softmax(scores, axis=-1)
        o = torch.einsum("bhqk,bhkd->bhqd", p, v_cache)
        return (lambda x, *d: x.permute(*d))(o, 0, 2, 1, 3).reshape(B, Sq, H * Dh) @ wo

    def _B_ref(x, w_gate, w_up, w_down):
        g = torch.einsum("bsd,df->bsf", x, w_gate)
        u = torch.einsum("bsd,df->bsf", x, w_up)
        h = g * u
        return torch.einsum("bsf,fd->bsd", h, w_down)

    x = A_x
    xn = _layernorm(x, axis=-1)
    return x + _A_ref(xn, A_k_cache, A_v_cache, A_wq, A_wo) + _B_ref(xn, B_w_gate, B_w_up, B_w_down)
```

```cpp
#include <hip/hip_runtime.h>
#include <hip/hip_bf16.h>
#include <stdint.h>

namespace {

constexpr int NB = 4, DM = 640, NH = 32, SK = 4096, DH = 128, FF = 1728;
constexpr int NWG = 256, NTHR = 512;

typedef unsigned short u16;
typedef unsigned u32x4 __attribute__((ext_vector_type(4)));
typedef unsigned u32x2 __attribute__((ext_vector_type(2)));
typedef float f32x4 __attribute__((ext_vector_type(4)));
typedef float f32x2 __attribute__((ext_vector_type(2)));
typedef __bf16 bf16x2 __attribute__((ext_vector_type(2)));
typedef __amdgpu_buffer_rsrc_t rsrc_t;

constexpr int AUX_NT = 2, AUX_SC1 = 16, RSRC_W3 = 0x00020000;

constexpr int WS_EPOCH = 0, WS_REC = 4096, WS_SLAB = WS_REC + NB * NH * 16 * 16, WS_END = WS_SLAB + NB * 64 * DM * 4;
constexpr int WS_STAMP = 1 << 20;
constexpr unsigned SENT = 0xFFFFFFFFu;
constexpr unsigned SPIN_CAP = 1u << 18;

constexpr int L_XN = 0;
constexpr int L_QP = L_XN + 8 * 640;
constexpr int L_GU = L_QP + 8 * 128;
constexpr int L_HH = L_GU + 8 * 64;
constexpr int L_OP = L_HH + 8 * 48;
constexpr int L_OT = L_OP + 8 * 128;
constexpr int L_YP = L_OT + 8 * 144;
constexpr int L_CR = L_YP + 6 * 640;
constexpr int L_END = L_CR + 8 * 32;

__device__ __forceinline__ rsrc_t mk(const void* p, unsigned bytes) {
  return __builtin_amdgcn_make_buffer_rsrc(const_cast<void*>(p), 0, (int)bytes, RSRC_W3);
}
template <int AUX> __device__ __forceinline__ u32x4 ld16(rsrc_t r, int voff, int soff) {
  return __builtin_amdgcn_raw_buffer_load_b128(r, voff, soff, AUX);
}
__device__ __forceinline__ float lo_f(unsigned u) { return __builtin_bit_cast(float, u << 16); }
__device__ __forceinline__ float hi_f(unsigned u) { return __builtin_bit_cast(float, u & 0xffff0000u); }
__device__ __forceinline__ float bfr(float f) { return (float)(__bf16)f; }
__device__ __forceinline__ unsigned pk_bf16(float lo, float hi) {
  bf16x2 v = {(__bf16)lo, (__bf16)hi};
  return __builtin_bit_cast(unsigned, v);
}
__device__ __forceinline__ float dot2(unsigned a, unsigned b, float acc) {
  return __builtin_amdgcn_fdot2_f32_bf16(__builtin_bit_cast(bf16x2, a), __builtin_bit_cast(bf16x2, b), acc, false);
}
template <int CTRL> __device__ __forceinline__ float dpp(float x) {
  return __builtin_bit_cast(float, __builtin_amdgcn_mov_dpp(__builtin_bit_cast(int, x), CTRL, 0xf, 0xf, true));
}
constexpr int XOR1 = 0xB1, XOR2 = 0x4E, XOR7 = 0x141, ROR8 = 0x128, ROR4 = 0x124, BCAST = 0x150;
__device__ __forceinline__ float fold32(float a, float b) {
  auto s = __builtin_amdgcn_permlane32_swap(__builtin_bit_cast(unsigned, a), __builtin_bit_cast(unsigned, b), false, false);
  return __builtin_bit_cast(float, (unsigned)s[0]) + __builtin_bit_cast(float, (unsigned)s[1]);
}
__device__ __forceinline__ float fold16(float a, float b) {
  auto s = __builtin_amdgcn_permlane16_swap(__builtin_bit_cast(unsigned, a), __builtin_bit_cast(unsigned, b), false, false);
  return __builtin_bit_cast(float, (unsigned)s[0]) + __builtin_bit_cast(float, (unsigned)s[1]);
}
__device__ __forceinline__ float row_sum(float v) {
  v += dpp<XOR1>(v); v += dpp<XOR2>(v); v += dpp<XOR7>(v); v += dpp<ROR8>(v); return v;
}
__device__ __forceinline__ float row_max(float v) {
  v = fmaxf(v, dpp<XOR1>(v)); v = fmaxf(v, dpp<XOR2>(v)); v = fmaxf(v, dpp<XOR7>(v)); v = fmaxf(v, dpp<ROR8>(v)); return v;
}
__device__ __forceinline__ float wave_sum(float v) { v = row_sum(v); v = fold16(v, v); return fold32(v, v); }
__device__ __forceinline__ float wave_max(float v) {
  v = row_max(v);
  auto s = __builtin_amdgcn_permlane16_swap(__builtin_bit_cast(unsigned, v), __builtin_bit_cast(unsigned, v), false, false);
  v = fmaxf(__builtin_bit_cast(float, (unsigned)s[0]), __builtin_bit_cast(float, (unsigned)s[1]));
  auto t = __builtin_amdgcn_permlane32_swap(__builtin_bit_cast(unsigned, v), __builtin_bit_cast(unsigned, v), false, false);
  return fmaxf(__builtin_bit_cast(float, (unsigned)t[0]), __builtin_bit_cast(float, (unsigned)t[1]));
}
struct Acc8 {
  f32x2 v[4];
  __device__ __forceinline__ void zero() { v[0] = v[1] = v[2] = v[3] = f32x2{0.f, 0.f}; }
  __device__ __forceinline__ float operator[](int i) const { return v[i >> 1][i & 1]; }
  __device__ __forceinline__ void pin() { asm volatile("" : "+v"(v[0]), "+v"(v[1]), "+v"(v[2]), "+v"(v[3])); }
};
__device__ __forceinline__ void fma8(Acc8& acc, float s, u32x4 v) {
  const f32x2 ss = {s, s};
#pragma unroll
  for (int k = 0; k < 4; ++k) acc.v[k] = __builtin_elementwise_fma(ss, f32x2{lo_f(v[k]), hi_f(v[k])}, acc.v[k]);
}
#define MEMBAR() asm volatile("" ::: "memory")
#define SB() __builtin_amdgcn_sched_barrier(0)
#define WAVE_SYNC() do { __builtin_amdgcn_fence(__ATOMIC_RELEASE, "wavefront"); __builtin_amdgcn_wave_barrier(); __builtin_amdgcn_fence(__ATOMIC_ACQUIRE, "wavefront"); } while (0)

#define STAMP(i) do {} while (0)

__device__ __forceinline__ float k_turn(u32x4 (&a)[16], u32x4 q, rsrc_t rn, int sn, int voff, int c) {
  float x[16];
#pragma unroll
  for (int t = 0; t < 16; ++t) {
    float d = dot2(a[t][0], q[0], 0.f);
    d = dot2(a[t][1], q[1], d); d = dot2(a[t][2], q[2], d); d = dot2(a[t][3], q[3], d);
    x[t] = d;
    a[t] = ld16<AUX_NT>(rn, voff + (t & 3) * 1024, sn + (t >> 2) * 4096);
    SB();
  }
#define TR_STEP(HALF, CTRL) _Pragma("unroll") for (int i = 0; i < (HALF); ++i) { \
    const float lo_ = x[i], hi_ = x[i + (HALF)]; const float t1_ = lo_ + dpp<CTRL>(lo_), t2_ = hi_ + dpp<CTRL>(hi_); \
    x[i] = (c & (HALF)) ? t2_ : t1_; }
  TR_STEP(8, ROR8) TR_STEP(4, XOR7) TR_STEP(2, XOR2) TR_STEP(1, XOR1)
#undef TR_STEP
  return x[0];
}

template <int T> struct VStep {
  static __device__ __forceinline__ void run(u32x4 (&a)[16], Acc8& acc, float p, rsrc_t rv, int sn, int voff) {
    fma8(acc, dpp<BCAST + T>(p), a[T]);
    acc.pin();
    a[T] = ld16<AUX_NT>(rv, voff + (T & 3) * 1024, sn + (T >> 2) * 4096);
    SB();
    VStep<T + 1>::run(a, acc, p, rv, sn, voff);
  }
};
template <> struct VStep<16> {
  static __device__ __forceinline__ void run(u32x4 (&)[16], Acc8&, float, rsrc_t, int, int) {}
};

__global__ void __launch_bounds__(NTHR, 2)
fused_decode(const u16* __restrict__ x, const u16* __restrict__ kc, const u16* __restrict__ vc, const u16* __restrict__ wq,
             const u16* __restrict__ wo, const u16* __restrict__ wg, const u16* __restrict__ wu, const u16* __restrict__ wd,
             u16* __restrict__ out, unsigned char* ws) {
  __shared__ __attribute__((aligned(16))) float lds[L_END];
  const int tid = threadIdx.x, lane = tid & 63, w = __builtin_amdgcn_readfirstlane(tid >> 6);
  const int bid = blockIdx.x, j = bid >> 3;
  const int h = (bid & 7) * 4 + (j >> 3), b = (j & 7) >> 1, s = j & 1;
  const int pidx = 2 * h + s;
  const bool ffn = pidx < FF / 32;
  const int row = lane >> 4, c = lane & 15;
  const rsrc_t rws = mk(ws, WS_END);
  STAMP(0);

  const unsigned epoch = __builtin_amdgcn_raw_buffer_load_b32(rws, WS_EPOCH + b * 256, 0, AUX_SC1);
  const int slab = WS_SLAB + (b * 64 + pidx) * (DM * 4);
  if (w == 7) {
    const u32x4 sv = {SENT, SENT, SENT, SENT};
    __builtin_amdgcn_raw_buffer_store_b128(sv, rws, lane * 16, slab, AUX_SC1);
    __builtin_amdgcn_raw_buffer_store_b128(sv, rws, lane * 16 + 1024, slab, AUX_SC1);
    if (lane < 32) __builtin_amdgcn_raw_buffer_store_b128(sv, rws, lane * 16 + 2048, slab, AUX_SC1);
    asm volatile("s_waitcnt vmcnt(0)" ::: "memory");
  }

  const rsrc_t rx = mk(x + b * DM, DM * 2);
  unsigned xr[5];
#pragma unroll
  for (int i = 0; i < 5; ++i) xr[i] = __builtin_amdgcn_raw_buffer_load_b32(rx, lane * 4 + i * 256, 0, 0);
  u32x4 wbuf[28];
  {
    const rsrc_t rq = mk(wq + h * DH, DM * NH * DH * 2 - h * DH * 2);
    const int vo = (tid >> 4) * (20 * 8192) + c * 16;
#pragma unroll
    for (int i = 0; i < 20; ++i) wbuf[i] = ld16<0>(rq, vo, i * 8192);
  }
  u32x4 gbuf[10];
  {
    const unsigned nb = ffn ? (unsigned)(DM * FF * 2 - pidx * 64) : 0u;
    const int pc = ffn ? pidx : 0;
    const rsrc_t rg = mk(wg + pc * 32, nb), ru = mk(wu + pc * 32, nb);
    const int vo = (tid >> 2) * (FF * 2) + (tid & 3) * 16;
#pragma unroll
    for (int i = 0; i < 5; ++i) { gbuf[i] = ld16<0>(rg, vo, i * 128 * FF * 2); gbuf[5 + i] = ld16<0>(ru, vo, i * 128 * FF * 2); }
  }
  const size_t kvoff = ((size_t)(b * NH + h) * SK + s * 2048 + w * 256) * DH;
  const rsrc_t rk = mk(kc + kvoff, 65536), rv = mk(vc + kvoff, 65536);
  const int voff = lane * 16;
  u32x4 a[16];
#pragma unroll
  for (int t = 0; t < 16; ++t) a[t] = ld16<AUX_NT>(rk, voff + (t & 3) * 1024, (t >> 2) * 4096);
  SB();

  float* xn = lds + L_XN + w * 640;
  {
    float xv[10], sum = 0.f;
#pragma unroll
    for (int i = 0; i < 5; ++i) { xv[2 * i] = lo_f(xr[i]); xv[2 * i + 1] = hi_f(xr[i]); sum += xv[2 * i] + xv[2 * i + 1]; }
    const float mean = wave_sum(sum) * (1.f / DM);
    float sq = 0.f;
#pragma unroll
    for (int i = 0; i < 10; ++i) { const float d = xv[i] - mean; sq = fmaf(d, d, sq); }
    const float var = bfr(wave_sum(sq) * (1.f / DM));
    const float mb = bfr(mean), sd = bfr(sqrtf(bfr(var + 1e-5f)));
#pragma unroll
    for (int i = 0; i < 5; ++i) {
      f32x2 o = {bfr(bfr(xv[2 * i] - mb) / sd), bfr(bfr(xv[2 * i + 1] - mb) / sd)};
      *(f32x2*)(xn + 2 * lane + 128 * i) = o;
    }
  }
  WAVE_SYNC();
  {
    Acc8 acc; acc.zero();
#pragma unroll
    for (int i4 = 0; i4 < 5; ++i4) {
      const f32x4 xv = *(const f32x4*)(xn + (tid >> 4) * 20 + 4 * i4);
#pragma unroll
      for (int k = 0; k < 4; ++k) fma8(acc, xv[k], wbuf[4 * i4 + k]);
    }
    const float e0 = fold32(acc[0], acc[4]), e1 = fold32(acc[1], acc[5]), e2 = fold32(acc[2], acc[6]), e3 = fold32(acc[3], acc[7]);
    const f32x2 o = {fold16(e0, e2), fold16(e1, e3)};
    *(f32x2*)(lds + L_QP + w * 128 + 8 * c + 2 * row) = o;
  }
  {
    Acc8 ag, au; ag.zero(); au.zero();
#pragma unroll
    for (int i = 0; i < 5; ++i) { const float xv = xn[(tid >> 2) + 128 * i]; fma8(ag, xv, gbuf[i]); fma8(au, xv, gbuf[5 + i]); }
    float e[8];
#pragma unroll
    for (int k = 0; k < 8; ++k) e[k] = fold32(ag[k], au[k]);
    f32x4 o;
#pragma unroll
    for (int k = 0; k < 4; ++k) { float v = fold16(e[k], e[k + 4]); v += dpp<ROR8>(v); v += dpp<ROR4>(v); o[k] = v; }
    if (c < 4) *(f32x4*)(lds + L_GU + w * 64 + (row >> 1) * 32 + 8 * c + 4 * (row & 1)) = o;
  }
  const int pcs = tid < 480 ? tid * 16 : (1 << 28);
  {
    const rsrc_t ro = mk(wo + (size_t)h * DH * DM, DH * DM * 2);
    const rsrc_t rd = mk(wd + (size_t)(ffn ? pidx : 0) * 32 * DM, ffn ? 32 * DM * 2 : 0);
#pragma unroll
    for (int i = 0; i < 22; ++i) wbuf[i] = ld16<0>(ro, pcs, i * 7680);
#pragma unroll
    for (int i = 0; i < 6; ++i) wbuf[22 + i] = ld16<0>(rd, pcs, i * 7680);
  }
  SB();
  __syncthreads();
  u32x4 q;
  {
    f32x4 s0 = {0.f, 0.f, 0.f, 0.f}, s1 = {0.f, 0.f, 0.f, 0.f};
#pragma unroll
    for (int ww = 0; ww < 8; ++ww) { s0 += *(const f32x4*)(lds + L_QP + ww * 128 + 8 * c); s1 += *(const f32x4*)(lds + L_QP + ww * 128 + 8 * c + 4); }
    q[0] = pk_bf16(s0[0], s0[1]); q[1] = pk_bf16(s0[2], s0[3]); q[2] = pk_bf16(s1[0], s1[1]); q[3] = pk_bf16(s1[2], s1[3]);
    float gu = 0.f;
#pragma unroll
    for (int ww = 0; ww < 8; ++ww) gu += lds[L_GU + ww * 64 + lane];
    gu = bfr(gu);
    auto sw = __builtin_amdgcn_permlane32_swap(__builtin_bit_cast(unsigned, gu), __builtin_bit_cast(unsigned, gu), false, false);
    const float hv = bfr(__builtin_bit_cast(float, (unsigned)sw[0]) * __builtin_bit_cast(float, (unsigned)sw[1]));
    if (lane < 48) lds[L_HH + w * 48 + lane] = lane < 32 ? hv : 0.f;
  }
  STAMP(1);

  float sc0, sc1, sc2, sc3;
  sc3 = k_turn(a, q, rk, 16384, voff, c);
  sc0 = sc1 = sc2 = 0.f;
#pragma unroll 1
  for (int g = 1; g < 4; ++g) {
    const rsrc_t rn = mk((g < 3 ? kc : vc) + kvoff, 65536);
    const float v = k_turn(a, q, rn, g < 3 ? (g + 1) * 16384 : 0, voff, c);
    sc0 = sc1; sc1 = sc2; sc2 = sc3; sc3 = v;
  }
  STAMP(2);
  constexpr float SCALE = 0.08838834764831845f, LOG2E = 1.4426950408889634f;
  sc0 = bfr(bfr(sc0) * SCALE); sc1 = bfr(bfr(sc1) * SCALE); sc2 = bfr(bfr(sc2) * SCALE); sc3 = bfr(bfr(sc3) * SCALE);
  const float mw = wave_max(fmaxf(fmaxf(sc0, sc1), fmaxf(sc2, sc3)));
  float p0 = __builtin_amdgcn_exp2f((sc0 - mw) * LOG2E), p1 = __builtin_amdgcn_exp2f((sc1 - mw) * LOG2E);
  float p2 = __builtin_amdgcn_exp2f((sc2 - mw) * LOG2E), p3 = __builtin_amdgcn_exp2f((sc3 - mw) * LOG2E);
  const float lw = wave_sum((p0 + p1) + (p2 + p3));
  const unsigned tag0 = epoch + 1u, tag1 = (epoch + 1u) * 0x9E3779B1u + 0x7F4A7C15u;
  const int rec = WS_REC + (b * NH + h) * 256;
  if (lane == 0) {
    const u32x4 r = {__builtin_bit_cast(unsigned, mw), tag0, __builtin_bit_cast(unsigned, lw), tag1};
    __builtin_amdgcn_raw_buffer_store_b128(r, rws, (s * 8 + w) * 16, rec, AUX_SC1);
  }
  const int rr = tid / 80;
  Acc8 y; y.zero();
#pragma unroll
  for (int i = 0; i < 6; ++i) fma8(y, lds[L_HH + w * 48 + rr + 6 * i], wbuf[22 + i]);
  y.pin();
  SB();

  Acc8 acc; acc.zero();
  const int prec = WS_REC + (b * NH) * 256 + (tid >> 3) * 128 + 7 * 16;
  u32x4 r1, r2;
#pragma unroll 1
  for (int g = 0; g < 4; ++g) {
    MEMBAR();
    r1 = ld16<AUX_SC1>(rws, c * 16, rec);
    r2 = ld16<AUX_SC1>(rws, prec, 0);
    SB();
    VStep<0>::run(a, acc, p0, rv, (g + 1) * 16384, voff);
    p0 = p1; p1 = p2; p2 = p3;
  }
  asm volatile("" : "+v"(r1), "+v"(r2));
  STAMP(3);
  for (unsigned spin = 0; __builtin_amdgcn_ballot_w64(r1[1] != tag0 || r1[3] != tag1) != 0ull && spin < SPIN_CAP; ++spin) {
    __builtin_amdgcn_s_sleep(2); MEMBAR();
    r1 = ld16<AUX_SC1>(rws, c * 16, rec);
  }
  float fw;
  {
    const float mi = __uint_as_float(r1[0]), li = __uint_as_float(r1[2]);
    const float M = row_max(mi);
    const float L = row_sum(li * __builtin_amdgcn_exp2f((mi - M) * LOG2E));
    fw = __builtin_amdgcn_exp2f((mw - M) * LOG2E) / L;
  }
  {
    const float e0 = fold32(acc[0], acc[4]), e1 = fold32(acc[1], acc[5]), e2 = fold32(acc[2], acc[6]), e3 = fold32(acc[3], acc[7]);
    const f32x2 o = {fold16(e0, e2) * fw, fold16(e1, e3) * fw};
    *(f32x2*)(lds + L_OP + w * 128 + 8 * c + 2 * row) = o;
  }
  __syncthreads();
  {
    f32x2 o = {0.f, 0.f};
#pragma unroll
    for (int ww = 0; ww < 8; ++ww) o += *(const f32x2*)(lds + L_OP + ww * 128 + 2 * lane);
    float* ot = lds + L_OT + w * 144;
    const int d0 = 2 * lane, d1 = 2 * lane + 1;
    ot[(d0 % 6) * 24 + d0 / 6] = o[0];
    ot[(d1 % 6) * 24 + d1 / 6] = o[1];
    if (lane < 4) ot[(2 + lane) * 24 + 21] = 0.f;
    WAVE_SYNC();
    const float* orow = ot + (rr < 6 ? rr : 0) * 24;
#pragma unroll
    for (int i4 = 0; i4 < 6; ++i4) {
      const f32x4 ov = *(const f32x4*)(orow + 4 * i4);
#pragma unroll
      for (int k = 0; k < 4; ++k) if (4 * i4 + k < 22) fma8(y, ov[k], wbuf[4 * i4 + k]);
    }
    if (tid < 480) {
      *(f32x4*)(lds + L_YP + tid * 8) = f32x4{y[0], y[1], y[2], y[3]};
      *(f32x4*)(lds + L_YP + tid * 8 + 4) = f32x4{y[4], y[5], y[6], y[7]};
    }
  }
  __syncthreads();
  if (tid < 160) {
    f32x4 v = {0.f, 0.f, 0.f, 0.f};
#pragma unroll
    for (int i = 0; i < 6; ++i) v += *(const f32x4*)(lds + L_YP + i * 640 + tid * 4);
    u32x4 u = __builtin_bit_cast(u32x4, v);
#pragma unroll
    for (int k = 0; k < 4; ++k) u[k] = u[k] == SENT ? 0x7FC00000u : u[k];
    __builtin_amdgcn_raw_buffer_store_b128(u, rws, tid * 16, slab, AUX_SC1);
  }
  STAMP(4);

  if (pidx < 44) return;
  const int cg = pidx - 44;
  for (unsigned spin = 0; __builtin_amdgcn_ballot_w64(r2[1] != tag0 || r2[3] != tag1) != 0ull && spin < SPIN_CAP; ++spin) {
    __builtin_amdgcn_s_sleep(2); MEMBAR();
    r2 = ld16<AUX_SC1>(rws, prec, 0);
  }
  __builtin_amdgcn_fence(__ATOMIC_ACQUIRE, "wavefront");
  unsigned xo = 0;
  if (tid < 16) xo = __builtin_amdgcn_raw_buffer_load_b32(rx, (cg * 32 + 2 * tid) * 2, 0, 0);
  const int so = WS_SLAB + b * 64 * DM * 4 + cg * 128, vo = (tid >> 3) * (DM * 4) + (tid & 7) * 16;
  u32x4 pv = ld16<AUX_SC1>(rws, vo, so);
  for (unsigned spin = 0; __builtin_amdgcn_ballot_w64(pv[0] == SENT || pv[1] == SENT || pv[2] == SENT || pv[3] == SENT) != 0ull && spin < SPIN_CAP; ++spin) {
    __builtin_amdgcn_s_sleep(1); MEMBAR();
    pv = ld16<AUX_SC1>(rws, vo, so);
  }
  {
    const f32x4 f = __builtin_bit_cast(f32x4, pv);
    const float e0 = fold32(f[0], f[2]), e1 = fold32(f[1], f[3]);
    float v = fold16(e0, e1);
    v += dpp<ROR8>(v);
    if (c < 8) lds[L_CR + w * 32 + 4 * c + row] = v;
  }
  __syncthreads();
  if (tid < 16) {
    f32x2 v = {0.f, 0.f};
#pragma unroll
    for (int ww = 0; ww < 8; ++ww) v += *(const f32x2*)(lds + L_CR + ww * 32 + 2 * tid);
    *(unsigned*)(out + b * DM + cg * 32 + 2 * tid) = pk_bf16(lo_f(xo) + v[0], hi_f(xo) + v[1]);
    if (tid == 0 && cg == 0) __builtin_amdgcn_raw_buffer_store_b32(tag0, rws, WS_EPOCH + b * 256, 0, AUX_SC1);
  }
  STAMP(5);
}

}

extern "C" void kernel_launch(void* const* d_in, const int* in_sizes, int n_in, void* d_out, int out_size, void* d_ws, size_t ws_size,
                              hipStream_t stream) {
  (void)in_sizes; (void)n_in; (void)out_size; (void)ws_size;
  fused_decode<<<NWG, NTHR, 0, stream>>>((const u16*)d_in[0], (const u16*)d_in[1], (const u16*)d_in[2], (const u16*)d_in[3],
                                         (const u16*)d_in[4], (const u16*)d_in[6], (const u16*)d_in[7], (const u16*)d_in[8],
                                         (u16*)d_out, (unsigned char*)d_ws);
}
```

```cpp
#include <hip/hip_runtime.h>
#include <hip/hip_bf16.h>
#include <stdint.h>

namespace {

constexpr int NB = 4, DM = 640, NH = 32, SK = 4096, DH = 128, FF = 1728;
constexpr int NWG = 256, NTHR = 512;

typedef unsigned short u16;
typedef unsigned u32x4 __attribute__((ext_vector_type(4)));
typedef unsigned u32x2 __attribute__((ext_vector_type(2)));
typedef float f32x4 __attribute__((ext_vector_type(4)));
typedef float f32x2 __attribute__((ext_vector_type(2)));
typedef __bf16 bf16x2 __attribute__((ext_vector_type(2)));
typedef __amdgpu_buffer_rsrc_t rsrc_t;

constexpr int AUX_K = 2, AUX_V = 0, AUX_W = 0, AUX_NT = 2, AUX_SC1 = 16, RSRC_W3 = 0x00020000;

constexpr int WS_EPOCH = 0, WS_REC = 4096, WS_SLAB = WS_REC + NB * NH * 16 * 16, WS_END = WS_SLAB + NB * 64 * DM * 4;
constexpr int WS_STAMP = 1 << 20;
constexpr unsigned SENT = 0xFFFFFFFFu;
constexpr unsigned SPIN_CAP = 1u << 18;

constexpr int L_XN = 0;
constexpr int L_QP = L_XN + 8 * 640;
constexpr int L_GU = L_QP + 8 * 128;
constexpr int L_HH = L_GU + 8 * 64;
constexpr int L_OP = L_HH + 8 * 48;
constexpr int L_OT = L_OP + 8 * 128;
constexpr int L_YP = L_OT + 8 * 144;
constexpr int L_CR = L_YP + 6 * 640;
constexpr int L_END = L_CR + 8 * 32;

__device__ __forceinline__ rsrc_t mk(const void* p, unsigned bytes) {
  return __builtin_amdgcn_make_buffer_rsrc(const_cast<void*>(p), 0, (int)bytes, RSRC_W3);
}
template <int AUX> __device__ __forceinline__ u32x4 ld16(rsrc_t r, int voff, int soff) {
  return __builtin_amdgcn_raw_buffer_load_b128(r, voff, soff, AUX);
}
__device__ __forceinline__ float lo_f(unsigned u) { return __builtin_bit_cast(float, u << 16); }
__device__ __forceinline__ float hi_f(unsigned u) { return __builtin_bit_cast(float, u & 0xffff0000u); }
__device__ __forceinline__ float bfr(float f) { return (float)(__bf16)f; }
__device__ __forceinline__ unsigned pk_bf16(float lo, float hi) {
  bf16x2 v = {(__bf16)lo, (__bf16)hi};
  return __builtin_bit_cast(unsigned, v);
}
__device__ __forceinline__ float dot2(unsigned a, unsigned b, float acc) {
  return __builtin_amdgcn_fdot2_f32_bf16(__builtin_bit_cast(bf16x2, a), __builtin_bit_cast(bf16x2, b), acc, false);
}
template <int CTRL> __device__ __forceinline__ float dpp(float x) {
  return __builtin_bit_cast(float, __builtin_amdgcn_mov_dpp(__builtin_bit_cast(int, x), CTRL, 0xf, 0xf, true));
}
constexpr int XOR1 = 0xB1, XOR2 = 0x4E, XOR7 = 0x141, ROR8 = 0x128, ROR4 = 0x124, BCAST = 0x150;
__device__ __forceinline__ float fold32(float a, float b) {
  auto s = __builtin_amdgcn_permlane32_swap(__builtin_bit_cast(unsigned, a), __builtin_bit_cast(unsigned, b), false, false);
  return __builtin_bit_cast(float, (unsigned)s[0]) + __builtin_bit_cast(float, (unsigned)s[1]);
}
__device__ __forceinline__ float fold16(float a, float b) {
  auto s = __builtin_amdgcn_permlane16_swap(__builtin_bit_cast(unsigned, a), __builtin_bit_cast(unsigned, b), false, false);
  return __builtin_bit_cast(float, (unsigned)s[0]) + __builtin_bit_cast(float, (unsigned)s[1]);
}
__device__ __forceinline__ float row_sum(float v) {
  v += dpp<XOR1>(v); v += dpp<XOR2>(v); v += dpp<XOR7>(v); v += dpp<ROR8>(v); return v;
}
__device__ __forceinline__ float row_max(float v) {
  v = fmaxf(v, dpp<XOR1>(v)); v = fmaxf(v, dpp<XOR2>(v)); v = fmaxf(v, dpp<XOR7>(v)); v = fmaxf(v, dpp<ROR8>(v)); return v;
}
__device__ __forceinline__ float wave_sum(float v) { v = row_sum(v); v = fold16(v, v); return fold32(v, v); }
__device__ __forceinline__ float wave_max(float v) {
  v = row_max(v);
  auto s = __builtin_amdgcn_permlane16_swap(__builtin_bit_cast(unsigned, v), __builtin_bit_cast(unsigned, v), false, false);
  v = fmaxf(__builtin_bit_cast(float, (unsigned)s[0]), __builtin_bit_cast(float, (unsigned)s[1]));
  auto t = __builtin_amdgcn_permlane32_swap(__builtin_bit_cast(unsigned, v), __builtin_bit_cast(unsigned, v), false, false);
  return fmaxf(__builtin_bit_cast(float, (unsigned)t[0]), __builtin_bit_cast(float, (unsigned)t[1]));
}
struct Acc8 {
  f32x2 v[4];
  __device__ __forceinline__ void zero() { v[0] = v[1] = v[2] = v[3] = f32x2{0.f, 0.f}; }
  __device__ __forceinline__ float operator[](int i) const { return v[i >> 1][i & 1]; }
  __device__ __forceinline__ void pin() { asm volatile("" : "+v"(v[0]), "+v"(v[1]), "+v"(v[2]), "+v"(v[3])); }
};
__device__ __forceinline__ void fma8(Acc8& acc, float s, u32x4 v) {
  const f32x2 ss = {s, s};
#pragma unroll
  for (int k = 0; k < 4; ++k) acc.v[k] = __builtin_elementwise_fma(ss, f32x2{lo_f(v[k]), hi_f(v[k])}, acc.v[k]);
}
#define MEMBAR() asm volatile("" ::: "memory")
#define SB() __builtin_amdgcn_sched_barrier(0)
#define WAVE_SYNC() do { __builtin_amdgcn_fence(__ATOMIC_RELEASE, "wavefront"); __builtin_amdgcn_wave_barrier(); __builtin_amdgcn_fence(__ATOMIC_ACQUIRE, "wavefront"); } while (0)

#define STAMP(i) do {} while (0)

template <int I> struct IC { static constexpr int value = I; };
template <int I, int N, class F> __device__ __forceinline__ void static_for(F&& f) {
  if constexpr (I < N) { f(IC<I>{}); static_for<I + 1, N>(f); }
}
__device__ __forceinline__ float transpose_reduce(float (&x)[16], int c) {
#define TR_STEP(HALF, CTRL) _Pragma("unroll") for (int i = 0; i < (HALF); ++i) { \
    const float lo_ = x[i], hi_ = x[i + (HALF)]; const float t1_ = lo_ + dpp<CTRL>(lo_), t2_ = hi_ + dpp<CTRL>(hi_); \
    x[i] = (c & (HALF)) ? t2_ : t1_; }
  TR_STEP(8, ROR8) TR_STEP(4, XOR7) TR_STEP(2, XOR2) TR_STEP(1, XOR1)
#undef TR_STEP
  return x[0];
}

__global__ void __launch_bounds__(NTHR, 2)
fused_decode(const u16* __restrict__ x, const u16* __restrict__ kc, const u16* __restrict__ vc, const u16* __restrict__ wq,
             const u16* __restrict__ wo, const u16* __restrict__ wg, const u16* __restrict__ wu, const u16* __restrict__ wd,
             u16* __restrict__ out, unsigned char* ws) {
  __shared__ __attribute__((aligned(16))) float lds[L_END];
  const int tid = threadIdx.x, lane = tid & 63, w = __builtin_amdgcn_readfirstlane(tid >> 6);
  const int bid = blockIdx.x, j = bid >> 3;
  const int h = (bid & 7) * 4 + (j >> 3), b = (j & 7) >> 1, s = j & 1;
  const int pidx = 2 * h + s;
  const bool ffn = pidx < FF / 32;
  const int row = lane >> 4, c = lane & 15;
  const rsrc_t rws = mk(ws, WS_END);
  STAMP(0);

  const unsigned epoch = __builtin_amdgcn_raw_buffer_load_b32(rws, WS_EPOCH + b * 256, 0, AUX_SC1);
  const int slab = WS_SLAB + (b * 64 + pidx) * (DM * 4);
  if (w == 7) {
    const u32x4 sv = {SENT, SENT, SENT, SENT};
    __builtin_amdgcn_raw_buffer_store_b128(sv, rws, lane * 16, slab, AUX_SC1);
    __builtin_amdgcn_raw_buffer_store_b128(sv, rws, lane * 16 + 1024, slab, AUX_SC1);
    if (lane < 32) __builtin_amdgcn_raw_buffer_store_b128(sv, rws, lane * 16 + 2048, slab, AUX_SC1);
    asm volatile("s_waitcnt vmcnt(0)" ::: "memory");
  }

  const rsrc_t rx = mk(x + b * DM, DM * 2);
  const rsrc_t rq = mk(wq + h * DH, DM * NH * DH * 2 - h * DH * 2);
  const int vq = (tid >> 4) * (20 * 8192) + c * 16;
  const unsigned nbg = ffn ? (unsigned)(DM * FF * 2 - pidx * 64) : 0u;
  const int pc = ffn ? pidx : 0;
  const rsrc_t rg = mk(wg + pc * 32, nbg), ru = mk(wu + pc * 32, nbg);
  const int vg = (tid >> 2) * (FF * 2) + (tid & 3) * 16;
  const rsrc_t ro = mk(wo + (size_t)h * DH * DM, DH * DM * 2);
  const rsrc_t rd = mk(wd + (size_t)pc * 32 * DM, ffn ? 32 * DM * 2 : 0);
  const int pcs = tid < 480 ? tid * 16 : (1 << 28);
  const size_t kvoff = ((size_t)(b * NH + h) * SK + s * 2048 + w * 256) * DH;
  const rsrc_t rk = mk(kc + kvoff, 65536), rv = mk(vc + kvoff, 65536);
  const int voff = lane * 16;
  u32x4 kv[128], wqb[20], gbuf[10], wdb[6], wob[22];
#define LD_KV(n) do { if ((n) < 64) kv[n] = ld16<AUX_K>(rk, voff + ((n) & 3) * 1024, (((n) & 63) >> 2) * 4096); else kv[n] = ld16<AUX_V>(rv, voff + ((n) & 3) * 1024, (((n) & 63) >> 2) * 4096); } while (0)
#define LD_WQ(i) do { wqb[i] = ld16<AUX_W>(rq, vq, (i) * 8192); } while (0)

  unsigned xr[5];
#pragma unroll
  for (int i = 0; i < 5; ++i) xr[i] = __builtin_amdgcn_raw_buffer_load_b32(rx, lane * 4 + i * 256, 0, AUX_W);
  constexpr int WQR = 5;
#pragma unroll
  for (int i = 0; i < WQR; ++i) { LD_WQ(i); SB(); LD_KV(i); SB(); }

  float* xn = lds + L_XN + w * 640;
  {
    float xv[10], sum = 0.f;
#pragma unroll
    for (int i = 0; i < 5; ++i) { xv[2 * i] = lo_f(xr[i]); xv[2 * i + 1] = hi_f(xr[i]); sum += xv[2 * i] + xv[2 * i + 1]; }
    const float mean = wave_sum(sum) * (1.f / DM);
    float sq = 0.f;
#pragma unroll
    for (int i = 0; i < 10; ++i) { const float d = xv[i] - mean; sq = fmaf(d, d, sq); }
    const float var = bfr(wave_sum(sq) * (1.f / DM));
    const float mb = bfr(mean), sd = bfr(sqrtf(bfr(var + 1e-5f)));
#pragma unroll
    for (int i = 0; i < 5; ++i) {
      f32x2 o = {bfr(bfr(xv[2 * i] - mb) / sd), bfr(bfr(xv[2 * i + 1] - mb) / sd)};
      *(f32x2*)(xn + 2 * lane + 128 * i) = o;
    }
  }
  WAVE_SYNC();
  {
    Acc8 acc; acc.zero();
    f32x4 xv[5];
#pragma unroll
    for (int i4 = 0; i4 < 5; ++i4) xv[i4] = *(const f32x4*)(xn + (tid >> 4) * 20 + 4 * i4);
    SB();
#pragma unroll
    for (int i = 0; i < 20; ++i) {
      fma8(acc, xv[i >> 2][i & 3], wqb[i]);
      acc.pin();
      if (i + WQR < 20) LD_WQ(i + WQR);
      SB();
      LD_KV(i + WQR);
      SB();
    }
#pragma unroll
    for (int n = 20 + WQR; n < 32; ++n) { LD_KV(n); SB(); }
    const float e0 = fold32(acc[0], acc[4]), e1 = fold32(acc[1], acc[5]), e2 = fold32(acc[2], acc[6]), e3 = fold32(acc[3], acc[7]);
    const f32x2 o = {fold16(e0, e2), fold16(e1, e3)};
    *(f32x2*)(lds + L_QP + w * 128 + 8 * c + 2 * row) = o;
  }
  __syncthreads();
  u32x4 q;
  {
    f32x4 s0 = {0.f, 0.f, 0.f, 0.f}, s1 = {0.f, 0.f, 0.f, 0.f};
#pragma unroll
    for (int ww = 0; ww < 8; ++ww) { s0 += *(const f32x4*)(lds + L_QP + ww * 128 + 8 * c); s1 += *(const f32x4*)(lds + L_QP + ww * 128 + 8 * c + 4); }
    q[0] = pk_bf16(s0[0], s0[1]); q[1] = pk_bf16(s0[2], s0[3]); q[2] = pk_bf16(s1[0], s1[1]); q[3] = pk_bf16(s1[2], s1[3]);
  }
  STAMP(1);

  float sc[4];
  {
    float xx[16];
#pragma unroll
    for (int st = 0; st < 64; ++st) {
      float d = dot2(kv[st][0], q[0], 0.f);
      d = dot2(kv[st][1], q[1], d); d = dot2(kv[st][2], q[2], d); d = dot2(kv[st][3], q[3], d);
      asm volatile("" : "+v"(d));
      xx[st & 15] = d;
      if ((st & 3) == 3) {
        const int i = st >> 2;
        if (i < 5) gbuf[i] = ld16<AUX_W>(rg, vg, i * 128 * FF * 2);
        else if (i < 10) gbuf[i] = ld16<AUX_W>(ru, vg, (i - 5) * 128 * FF * 2);
        else wdb[i - 10] = ld16<AUX_W>(rd, pcs, (i - 10) * 7680);
      } else {
        LD_KV(32 + st - (st >> 2));
      }
      SB();
      if ((st & 15) == 15) { sc[st >> 4] = transpose_reduce(xx, c); SB(); }
    }
  }
  STAMP(2);
  constexpr float SCALE = 0.08838834764831845f, LOG2E = 1.4426950408889634f;
#pragma unroll
  for (int g = 0; g < 4; ++g) sc[g] = bfr(bfr(sc[g]) * SCALE);
  const float mw = wave_max(fmaxf(fmaxf(sc[0], sc[1]), fmaxf(sc[2], sc[3])));
  float p[4];
#pragma unroll
  for (int g = 0; g < 4; ++g) p[g] = __builtin_amdgcn_exp2f((sc[g] - mw) * LOG2E);
  const float lw = wave_sum((p[0] + p[1]) + (p[2] + p[3]));
  const unsigned tag0 = epoch + 1u, tag1 = (epoch + 1u) * 0x9E3779B1u + 0x7F4A7C15u;
  const int rec = WS_REC + (b * NH + h) * 256;
  if (lane == 0) {
    const u32x4 r = {__float_as_uint(mw), tag0, __float_as_uint(lw), tag1};
    __builtin_amdgcn_raw_buffer_store_b128(r, rws, (s * 8 + w) * 16, rec, AUX_SC1);
  }
  {
    Acc8 ag, au; ag.zero(); au.zero();
#pragma unroll
    for (int i = 0; i < 5; ++i) { const float xv = xn[(tid >> 2) + 128 * i]; fma8(ag, xv, gbuf[i]); fma8(au, xv, gbuf[5 + i]); }
    float e[8];
#pragma unroll
    for (int k = 0; k < 8; ++k) e[k] = fold32(ag[k], au[k]);
    f32x4 o;
#pragma unroll
    for (int k = 0; k < 4; ++k) { float v = fold16(e[k], e[k + 4]); v += dpp<ROR8>(v); v += dpp<ROR4>(v); o[k] = v; }
    if (c < 4) *(f32x4*)(lds + L_GU + w * 64 + (row >> 1) * 32 + 8 * c + 4 * (row & 1)) = o;
  }
  __syncthreads();
  const int rr = tid / 80;
  Acc8 y; y.zero();
  {
    float gu = 0.f;
#pragma unroll
    for (int ww = 0; ww < 8; ++ww) gu += lds[L_GU + ww * 64 + lane];
    gu = bfr(gu);
    auto sw = __builtin_amdgcn_permlane32_swap(__float_as_uint(gu), __float_as_uint(gu), false, false);
    const float hv = bfr(__uint_as_float(sw[0]) * __uint_as_float(sw[1]));
    if (lane < 48) lds[L_HH + w * 48 + lane] = lane < 32 ? hv : 0.f;
    WAVE_SYNC();
#pragma unroll
    for (int i = 0; i < 6; ++i) fma8(y, lds[L_HH + w * 48 + rr + 6 * i], wdb[i]);
    y.pin();
  }
  SB();

  Acc8 acc; acc.zero();
  const int prec = WS_REC + (b * NH) * 256 + (tid >> 3) * 128 + 7 * 16;
  u32x4 r1, r2;
  static_for<0, 64>([&](auto I) {
    constexpr int st = decltype(I)::value;
    fma8(acc, dpp<BCAST + (st & 15)>(p[st >> 4]), kv[64 + st]);
    acc.pin();
    if constexpr (st < 48) LD_KV(64 + 16 + st);
    SB();
    if constexpr ((st & 1) == 1 && (st >> 1) < 22) { wob[st >> 1] = ld16<AUX_W>(ro, pcs, (st >> 1) * 7680); SB(); }
    if constexpr (st == 47) { MEMBAR(); r1 = ld16<AUX_SC1>(rws, c * 16, rec); r2 = ld16<AUX_SC1>(rws, prec, 0); SB(); }
  });
  asm volatile("" : "+v"(r1), "+v"(r2));
  STAMP(3);
  for (unsigned spin = 0; __builtin_amdgcn_ballot_w64(r1[1] != tag0 || r1[3] != tag1) != 0ull && spin < SPIN_CAP; ++spin) {
    __builtin_amdgcn_s_sleep(2); MEMBAR();
    r1 = ld16<AUX_SC1>(rws, c * 16, rec);
  }
  float fw;
  {
    const float mi = __uint_as_float(r1[0]), li = __uint_as_float(r1[2]);
    const float M = row_max(mi);
    const float L = row_sum(li * __builtin_amdgcn_exp2f((mi - M) * LOG2E));
    fw = __builtin_amdgcn_exp2f((mw - M) * LOG2E) / L;
  }
  {
    const float e0 = fold32(acc[0], acc[4]), e1 = fold32(acc[1], acc[5]), e2 = fold32(acc[2], acc[6]), e3 = fold32(acc[3], acc[7]);
    const f32x2 o = {fold16(e0, e2) * fw, fold16(e1, e3) * fw};
    *(f32x2*)(lds + L_OP + w * 128 + 8 * c + 2 * row) = o;
  }
  __syncthreads();
  {
    f32x2 o = {0.f, 0.f};
#pragma unroll
    for (int ww = 0; ww < 8; ++ww) o += *(const f32x2*)(lds + L_OP + ww * 128 + 2 * lane);
    float* ot = lds + L_OT + w * 144;
    const int d0 = 2 * lane, d1 = 2 * lane + 1;
    ot[(d0 % 6) * 24 + d0 / 6] = o[0];
    ot[(d1 % 6) * 24 + d1 / 6] = o[1];
    if (lane < 4) ot[(2 + lane) * 24 + 21] = 0.f;
    WAVE_SYNC();
    const float* orow = ot + (rr < 6 ? rr : 0) * 24;
#pragma unroll
    for (int i4 = 0; i4 < 6; ++i4) {
      const f32x4 ov = *(const f32x4*)(orow + 4 * i4);
#pragma unroll
      for (int k = 0; k < 4; ++k) if (4 * i4 + k < 22) fma8(y, ov[k], wob[4 * i4 + k]);
    }
    if (tid < 480) {
      *(f32x4*)(lds + L_YP + tid * 8) = f32x4{y[0], y[1], y[2], y[3]};
      *(f32x4*)(lds + L_YP + tid * 8 + 4) = f32x4{y[4], y[5], y[6], y[7]};
    }
  }
  __syncthreads();
  if (tid < 160) {
    f32x4 v = {0.f, 0.f, 0.f, 0.f};
#pragma unroll
    for (int i = 0; i < 6; ++i) v += *(const f32x4*)(lds + L_YP + i * 640 + tid * 4);
    u32x4 u = __builtin_bit_cast(u32x4, v);
#pragma unroll
    for (int k = 0; k < 4; ++k) u[k] = u[k] == SENT ? 0x7FC00000u : u[k];
    __builtin_amdgcn_raw_buffer_store_b128(u, rws, tid * 16, slab, AUX_SC1);
  }
  STAMP(4);

  if (pidx < 44) return;
  const int cg = pidx - 44;
  for (unsigned spin = 0; __builtin_amdgcn_ballot_w64(r2[1] != tag0 || r2[3] != tag1) != 0ull && spin < SPIN_CAP; ++spin) {
    __builtin_amdgcn_s_sleep(2); MEMBAR();
    r2 = ld16<AUX_SC1>(rws, prec, 0);
  }
  __builtin_amdgcn_fence(__ATOMIC_ACQUIRE, "wavefront");
  unsigned xo = 0;
  if (tid < 16) xo = __builtin_amdgcn_raw_buffer_load_b32(rx, (cg * 32 + 2 * tid) * 2, 0, AUX_W);
  const int so = WS_SLAB + b * 64 * DM * 4 + cg * 128, vo = (tid >> 3) * (DM * 4) + (tid & 7) * 16;
  u32x4 pv = ld16<AUX_SC1>(rws, vo, so);
  for (unsigned spin = 0; __builtin_amdgcn_ballot_w64(pv[0] == SENT || pv[1] == SENT || pv[2] == SENT || pv[3] == SENT) != 0ull && spin < SPIN_CAP; ++spin) {
    __builtin_amdgcn_s_sleep(1); MEMBAR();
    pv = ld16<AUX_SC1>(rws, vo, so);
  }
  {
    const f32x4 f = __builtin_bit_cast(f32x4, pv);
    const float e0 = fold32(f[0], f[2]), e1 = fold32(f[1], f[3]);
    float v = fold16(e0, e1);
    v += dpp<ROR8>(v);
    if (c < 8) lds[L_CR + w * 32 + 4 * c + row] = v;
  }
  __syncthreads();
  if (tid < 16) {
    f32x2 v = {0.f, 0.f};
#pragma unroll
    for (int ww = 0; ww < 8; ++ww) v += *(const f32x2*)(lds + L_CR + ww * 32 + 2 * tid);
    *(unsigned*)(out + b * DM + cg * 32 + 2 * tid) = pk_bf16(lo_f(xo) + v[0], hi_f(xo) + v[1]);
    if (tid == 0 && cg == 0) __builtin_amdgcn_raw_buffer_store_b32(tag0, rws, WS_EPOCH + b * 256, 0, AUX_SC1);
  }
  STAMP(5);
}

}

extern "C" void kernel_launch(void* const* d_in, const int* in_sizes, int n_in, void* d_out, int out_size, void* d_ws, size_t ws_size,
                              hipStream_t stream) {
  (void)in_sizes; (void)n_in; (void)out_size; (void)ws_size;
  fused_decode<<<NWG, NTHR, 0, stream>>>((const u16*)d_in[0], (const u16*)d_in[1], (const u16*)d_in[2], (const u16*)d_in[3],
                                         (const u16*)d_in[4], (const u16*)d_in[6], (const u16*)d_in[7], (const u16*)d_in[8],
                                         (u16*)d_out, (unsigned char*)d_ws);
}
```

```cpp
#include <hip/hip_runtime.h>
#include <hip/hip_bf16.h>
#include <stdint.h>

namespace {

constexpr int NB = 4, DM = 640, NH = 32, SK = 4096, DH = 128, FF = 1728;
constexpr int NWG = 256, NTHR = 512;
#define ROT_ON 1
#define PRIO 1
#define WQR 6
#define N2 20
#define NEXTRA 6
#define KOFF 10
#define FMAP 1

typedef unsigned short u16;
typedef unsigned u32x4 __attribute__((ext_vector_type(4)));
typedef unsigned u32x2 __attribute__((ext_vector_type(2)));
typedef float f32x4 __attribute__((ext_vector_type(4)));
typedef float f32x2 __attribute__((ext_vector_type(2)));
typedef __bf16 bf16x2 __attribute__((ext_vector_type(2)));
typedef __amdgpu_buffer_rsrc_t rsrc_t;

constexpr int AUX_K = 2, AUX_V = 2, AUX_W = 0, AUX_NT = 2, AUX_SC1 = 16, RSRC_W3 = 0x00020000;

constexpr int WS_EPOCH = 0, WS_REC = 4096, WS_SLAB = WS_REC + NB * NH * 16 * 16, WS_END = WS_SLAB + NB * 64 * DM * 4;
constexpr int WS_STAMP = 1 << 20;
constexpr unsigned SENT = 0xFFFFFFFFu;
constexpr unsigned SPIN_CAP = 1u << 18;

constexpr int L_XN = 0;
constexpr int L_QP = L_XN + 8 * 640;
constexpr int L_GU = L_QP + 8 * 128;
constexpr int L_HH = L_GU + 8 * 64;
constexpr int L_OP = L_HH + 8 * 48;
constexpr int L_OT = L_OP + 8 * 128;
constexpr int L_YP = L_OT + 8 * 72;
constexpr int L_END = L_YP + 6 * 640;

__device__ __forceinline__ rsrc_t mk(const void* p, unsigned bytes) {
  return __builtin_amdgcn_make_buffer_rsrc(const_cast<void*>(p), 0, (int)bytes, RSRC_W3);
}
template <int AUX> __device__ __forceinline__ u32x4 ld16(rsrc_t r, int voff, int soff) {
  return __builtin_amdgcn_raw_buffer_load_b128(r, voff, soff, AUX);
}
__device__ __forceinline__ float lo_f(unsigned u) { return __builtin_bit_cast(float, u << 16); }
__device__ __forceinline__ float hi_f(unsigned u) { return __builtin_bit_cast(float, u & 0xffff0000u); }
__device__ __forceinline__ float bfr(float f) { return (float)(__bf16)f; }
__device__ __forceinline__ unsigned pk_bf16(float lo, float hi) {
  bf16x2 v = {(__bf16)lo, (__bf16)hi};
  return __builtin_bit_cast(unsigned, v);
}
__device__ __forceinline__ float dot2(unsigned a, unsigned b, float acc) {
  return __builtin_amdgcn_fdot2_f32_bf16(__builtin_bit_cast(bf16x2, a), __builtin_bit_cast(bf16x2, b), acc, false);
}
template <int CTRL> __device__ __forceinline__ float dpp(float x) {
  return __builtin_bit_cast(float, __builtin_amdgcn_mov_dpp(__builtin_bit_cast(int, x), CTRL, 0xf, 0xf, true));
}
constexpr int XOR1 = 0xB1, XOR2 = 0x4E, XOR7 = 0x141, ROR8 = 0x128, ROR4 = 0x124, BCAST = 0x150;
__device__ __forceinline__ float fold32(float a, float b) {
  auto s = __builtin_amdgcn_permlane32_swap(__builtin_bit_cast(unsigned, a), __builtin_bit_cast(unsigned, b), false, false);
  return __builtin_bit_cast(float, (unsigned)s[0]) + __builtin_bit_cast(float, (unsigned)s[1]);
}
__device__ __forceinline__ float fold16(float a, float b) {
  auto s = __builtin_amdgcn_permlane16_swap(__builtin_bit_cast(unsigned, a), __builtin_bit_cast(unsigned, b), false, false);
  return __builtin_bit_cast(float, (unsigned)s[0]) + __builtin_bit_cast(float, (unsigned)s[1]);
}
__device__ __forceinline__ float row_sum(float v) {
  v += dpp<XOR1>(v); v += dpp<XOR2>(v); v += dpp<XOR7>(v); v += dpp<ROR8>(v); return v;
}
__device__ __forceinline__ float row_max(float v) {
  v = fmaxf(v, dpp<XOR1>(v)); v = fmaxf(v, dpp<XOR2>(v)); v = fmaxf(v, dpp<XOR7>(v)); v = fmaxf(v, dpp<ROR8>(v)); return v;
}
__device__ __forceinline__ float wave_sum(float v) { v = row_sum(v); v = fold16(v, v); return fold32(v, v); }
__device__ __forceinline__ float wave_max(float v) {
  v = row_max(v);
  auto s = __builtin_amdgcn_permlane16_swap(__builtin_bit_cast(unsigned, v), __builtin_bit_cast(unsigned, v), false, false);
  v = fmaxf(__builtin_bit_cast(float, (unsigned)s[0]), __builtin_bit_cast(float, (unsigned)s[1]));
  auto t = __builtin_amdgcn_permlane32_swap(__builtin_bit_cast(unsigned, v), __builtin_bit_cast(unsigned, v), false, false);
  return fmaxf(__builtin_bit_cast(float, (unsigned)t[0]), __builtin_bit_cast(float, (unsigned)t[1]));
}
struct Acc8 {
  f32x2 v[4];
  __device__ __forceinline__ void zero() { v[0] = v[1] = v[2] = v[3] = f32x2{0.f, 0.f}; }
  __device__ __forceinline__ float operator[](int i) const { return v[i >> 1][i & 1]; }
  __device__ __forceinline__ void pin() { asm volatile("" : "+v"(v[0]), "+v"(v[1]), "+v"(v[2]), "+v"(v[3])); }
};
__device__ __forceinline__ void fma8(Acc8& acc, float s, u32x4 v) {
  const f32x2 ss = {s, s};
#pragma unroll
  for (int k = 0; k < 4; ++k) acc.v[k] = __builtin_elementwise_fma(ss, f32x2{lo_f(v[k]), hi_f(v[k])}, acc.v[k]);
}
#define MEMBAR() asm volatile("" ::: "memory")
#define SB() __builtin_amdgcn_sched_barrier(0)
#define WAVE_SYNC() do { __builtin_amdgcn_fence(__ATOMIC_RELEASE, "wavefront"); __builtin_amdgcn_wave_barrier(); __builtin_amdgcn_fence(__ATOMIC_ACQUIRE, "wavefront"); } while (0)

#define STAMP(i) do {} while (0)
#define STAMP_OUT() do {} while (0)

template <int I> struct IC { static constexpr int value = I; };
template <int I, int N, class F> __device__ __forceinline__ void static_for(F&& f) {
  if constexpr (I < N) { f(IC<I>{}); static_for<I + 1, N>(f); }
}
__device__ __forceinline__ float transpose_reduce(float (&x)[16], int c) {
#define TR_STEP(HALF, CTRL) _Pragma("unroll") for (int i = 0; i < (HALF); ++i) { \
    const float lo_ = x[i], hi_ = x[i + (HALF)]; const float t1_ = lo_ + dpp<CTRL>(lo_), t2_ = hi_ + dpp<CTRL>(hi_); \
    x[i] = (c & (HALF)) ? t2_ : t1_; }
  TR_STEP(8, ROR8) TR_STEP(4, XOR7) TR_STEP(2, XOR2) TR_STEP(1, XOR1)
#undef TR_STEP
  return x[0];
}

__global__ void __launch_bounds__(NTHR, 2)
fused_decode(const u16* __restrict__ x, const u16* __restrict__ kc, const u16* __restrict__ vc, const u16* __restrict__ wq,
             const u16* __restrict__ wo, const u16* __restrict__ wg, const u16* __restrict__ wu, const u16* __restrict__ wd,
             u16* __restrict__ out, unsigned char* ws) {
  __shared__ __attribute__((aligned(16))) float lds[L_END];
  const int tid = threadIdx.x, lane = tid & 63, w = __builtin_amdgcn_readfirstlane(tid >> 6);
  const int bid = blockIdx.x, j = bid >> 3;
  const int h = (bid & 7) * 4 + (j >> 3), b = (j & 7) >> 1, s = j & 1;
  const int pidx = 2 * h + s;
  const int slot = 2 * (j >> 3) + s, gq = (bid & 7) >> 1;
  const int fb = (bid & 1) ? 32 + 2 * (gq + 4 * (slot >> 1)) + (slot & 1) : 8 * gq + slot;
  const bool ffn = fb < FF / 32;
  const int row = lane >> 4, c = lane & 15;
  const rsrc_t rws = mk(ws, WS_END);
  STAMP(0);

  const unsigned epoch = __builtin_amdgcn_raw_buffer_load_b32(rws, WS_EPOCH + b * 256, 0, AUX_SC1);
  const int slab = WS_SLAB + (b * 64 + pidx) * (DM * 4);
  if (w == 7) {
    const u32x4 sv = {SENT, SENT, SENT, SENT};
    __builtin_amdgcn_raw_buffer_store_b128(sv, rws, lane * 16, slab, AUX_SC1);
    __builtin_amdgcn_raw_buffer_store_b128(sv, rws, lane * 16 + 1024, slab, AUX_SC1);
    if (lane < 32) __builtin_amdgcn_raw_buffer_store_b128(sv, rws, lane * 16 + 2048, slab, AUX_SC1);
    asm volatile("s_waitcnt vmcnt(0)" ::: "memory");
  }

  const rsrc_t rx = mk(x + b * DM, DM * 2);
  const rsrc_t rq = mk(wq + h * DH, DM * NH * DH * 2 - h * DH * 2);
  const int vq = (tid >> 4) * (20 * 8192) + c * 16;
  const int pc = ffn ? fb : 0;
  const unsigned nbg = ffn ? (unsigned)(DM * FF * 2 - pc * 64) : 0u;
  const rsrc_t rg = mk(wg + pc * 32, nbg), ru = mk(wu + pc * 32, nbg);
  const int vg = (tid >> 2) * (FF * 2) + (tid & 3) * 16;
  const rsrc_t ro = mk(wo + (size_t)h * DH * DM, DH * DM * 2);
  const rsrc_t rd = mk(wd + (size_t)pc * 32 * DM, ffn ? 32 * DM * 2 : 0);
  const int rr = tid / 80;
  const int pcs = tid < 480 ? tid * 16 : (1 << 28);
  const int pco = tid < 480 ? tid * 16 + rr * 1280 : (1 << 28);
  const size_t kvoff = ((size_t)(b * NH + h) * SK + s * 2048 + w * 256) * DH;
  const rsrc_t rk = mk(kc + kvoff, 65536), rv = mk(vc + kvoff, 65536);
  const unsigned rot = ROT_ON ? (((unsigned)(bid * 8 + w) * 0x9E3779B1u) >> 16) & 63u : 0u;
  int voffs[4];
#pragma unroll
  for (int k = 0; k < 4; ++k) voffs[k] = lane * 16 + (int)((k ^ rot) & 3u) * 1024;
  const int rhi = (int)(rot >> 2) * 4096;
  u32x4 kv[128], wqb[20], gbuf[10], wdb[6], wob[22];
#define LD_KV(n) do { if ((n) < 64) kv[n] = ld16<AUX_K>(rk, voffs[(n) & 3], rhi ^ ((((n) & 63) >> 2) * 4096)); else kv[n] = ld16<AUX_V>(rv, voffs[(n) & 3], rhi ^ ((((n) & 63) >> 2) * 4096)); SB(); } while (0)
#define LD_WQ(i) do { wqb[i] = ld16<AUX_W>(rq, vq, (i) * 8192); SB(); \
    if ((i) < N2) { LD_KV(2 * (i)); LD_KV(2 * (i) + 1); } else LD_KV(N2 + (i)); } while (0)
  constexpr int PRE = N2 + 20 + NEXTRA;

  unsigned xr[5];
#pragma unroll
  for (int i = 0; i < 5; ++i) xr[i] = __builtin_amdgcn_raw_buffer_load_b32(rx, lane * 4 + i * 256, 0, AUX_W);
  if (PRIO) __builtin_amdgcn_s_setprio(2);
#pragma unroll
  for (int i = 0; i < WQR; ++i) LD_WQ(i);
  STAMP(6);

  float* xn = lds + L_XN + w * 640;
  {
    float xv[10], sum = 0.f;
#pragma unroll
    for (int i = 0; i < 5; ++i) { xv[2 * i] = lo_f(xr[i]); xv[2 * i + 1] = hi_f(xr[i]); sum += xv[2 * i] + xv[2 * i + 1]; }
    const float mean = wave_sum(sum) * (1.f / DM);
    float sq = 0.f;
#pragma unroll
    for (int i = 0; i < 10; ++i) { const float d = xv[i] - mean; sq = fmaf(d, d, sq); }
    const float var = bfr(wave_sum(sq) * (1.f / DM));
    const float mb = bfr(mean), sd = bfr(sqrtf(bfr(var + 1e-5f)));
#pragma unroll
    for (int i = 0; i < 5; ++i) {
      f32x2 o = {bfr(bfr(xv[2 * i] - mb) / sd), bfr(bfr(xv[2 * i + 1] - mb) / sd)};
      *(f32x2*)(xn + 2 * lane + 128 * i) = o;
    }
  }
  WAVE_SYNC();
  {
    Acc8 acc; acc.zero();
    f32x4 xv = *(const f32x4*)(xn + (tid >> 4) * 20);
    SB();
#pragma unroll
    for (int i = 0; i < 20; ++i) {
      fma8(acc, xv[i & 3], wqb[i]);
      acc.pin();
      if ((i & 3) == 3 && i < 19) xv = *(const f32x4*)(xn + (tid >> 4) * 20 + i + 1);
      SB();
      if (i + WQR < 20) LD_WQ(i + WQR);
      if (i + WQR == 19 && PRIO) __builtin_amdgcn_s_setprio(0);
    }
    const float e0 = fold32(acc[0], acc[4]), e1 = fold32(acc[1], acc[5]), e2 = fold32(acc[2], acc[6]), e3 = fold32(acc[3], acc[7]);
    const f32x2 o = {fold16(e0, e2), fold16(e1, e3)};
    *(f32x2*)(lds + L_QP + w * 128 + 8 * c + 2 * row) = o;
    STAMP(7);
    SB();
#pragma unroll
    for (int n = N2 + 20; n < PRE; ++n) LD_KV(n);
  }
  __syncthreads();
  u32x4 q;
  {
    f32x4 s0 = {0.f, 0.f, 0.f, 0.f}, s1 = {0.f, 0.f, 0.f, 0.f};
#pragma unroll
    for (int ww = 0; ww < 8; ++ww) { s0 += *(const f32x4*)(lds + L_QP + ww * 128 + 8 * c); s1 += *(const f32x4*)(lds + L_QP + ww * 128 + 8 * c + 4); }
    q[0] = pk_bf16(s0[0], s0[1]); q[1] = pk_bf16(s0[2], s0[3]); q[2] = pk_bf16(s1[0], s1[1]); q[3] = pk_bf16(s1[2], s1[3]);
  }
  STAMP(1);

  float sc[4];
  {
    float xx[16];
#pragma unroll
    for (int st = 0; st < 64; ++st) {
      float d = dot2(kv[st][0], q[0], 0.f);
      d = dot2(kv[st][1], q[1], d); d = dot2(kv[st][2], q[2], d); d = dot2(kv[st][3], q[3], d);
      asm volatile("" : "+v"(d));
      xx[st & 15] = d;
      const int u = st - KOFF;
      const int nx = u / 3 < 16 ? u / 3 : 16;
      if (u < 0) {
      } else if (u % 3 == 2 && u / 3 < 16) {
        const int i = u / 3;
        if (i < 5) gbuf[i] = ld16<AUX_W>(rg, vg, i * 128 * FF * 2);
        else if (i < 10) gbuf[i] = ld16<AUX_W>(ru, vg, (i - 5) * 128 * FF * 2);
        else wdb[i - 10] = ld16<AUX_W>(rd, pcs, (i - 10) * 7680);
      } else if (PRE + u - nx < 80) {
        LD_KV(PRE + u - nx);
      }
      SB();
      if ((st & 15) == 15) { sc[st >> 4] = transpose_reduce(xx, c); SB(); }
    }
  }
  static_assert(PRE + 63 - KOFF - 16 >= 79, "V0..V15 are all issued in the K phase");
  STAMP(2);
  constexpr float SCALE = 0.08838834764831845f, LOG2E = 1.4426950408889634f;
#pragma unroll
  for (int g = 0; g < 4; ++g) sc[g] = bfr(bfr(sc[g]) * SCALE);
  const float mw = wave_max(fmaxf(fmaxf(sc[0], sc[1]), fmaxf(sc[2], sc[3])));
  float p[4];
#pragma unroll
  for (int g = 0; g < 4; ++g) p[g] = __builtin_amdgcn_exp2f((sc[g] - mw) * LOG2E);
  const float lw = wave_sum((p[0] + p[1]) + (p[2] + p[3]));
  const unsigned tag0 = epoch + 1u, tag1 = (epoch + 1u) * 0x9E3779B1u + 0x7F4A7C15u;
  const int rec = WS_REC + (b * NH + h) * 256;
  if (lane == 0) {
    const u32x4 r = {__float_as_uint(mw), tag0, __float_as_uint(lw), tag1};
    __builtin_amdgcn_raw_buffer_store_b128(r, rws, (s * 8 + w) * 16, rec, AUX_SC1);
  }
  Acc8 y; y.zero();
  {
    Acc8 ag, au; ag.zero(); au.zero();
#pragma unroll
    for (int i = 0; i < 5; ++i) { const float xv = xn[(tid >> 2) + 128 * i]; fma8(ag, xv, gbuf[i]); fma8(au, xv, gbuf[5 + i]); }
    float e[8];
#pragma unroll
    for (int k = 0; k < 8; ++k) e[k] = fold32(ag[k], au[k]);
    f32x4 o;
#pragma unroll
    for (int k = 0; k < 4; ++k) { float v = fold16(e[k], e[k + 4]); v += dpp<ROR8>(v); v += dpp<ROR4>(v); o[k] = v; }
    if (c < 4) *(f32x4*)(lds + L_GU + w * 64 + (row >> 1) * 32 + 8 * c + 4 * (row & 1)) = o;
    __syncthreads();
    float gu = 0.f;
#pragma unroll
    for (int ww = 0; ww < 8; ++ww) gu += lds[L_GU + ww * 64 + lane];
    gu = bfr(gu);
    auto sw = __builtin_amdgcn_permlane32_swap(__float_as_uint(gu), __float_as_uint(gu), false, false);
    const float hv = bfr(__uint_as_float(sw[0]) * __uint_as_float(sw[1]));
    if (lane < 48) lds[L_HH + w * 48 + lane] = lane < 32 ? hv : 0.f;
  }
  WAVE_SYNC();
  SB();

  Acc8 acc; acc.zero();
  const int cg = pidx - 24;
  const bool red = w == 7 && cg >= 0;
  const int prec = red ? WS_REC + (b * NH) * 256 + lane * 128 + 7 * 16 : (1 << 28);
  u32x4 r1, r2;
  unsigned xo = 0;
  float fw = 0.f;
  bool ok2 = false;
  static_for<0, 64>([&](auto I) {
    constexpr int st = decltype(I)::value;
    fma8(acc, dpp<BCAST + (st & 15)>(p[st >> 4]), kv[64 + st]);
    acc.pin();
    if constexpr (st < 48) LD_KV(64 + 16 + st);
    SB();
    if constexpr ((st & 1) == 1 && (st >> 1) < 22) { wob[st >> 1] = ld16<AUX_W>(ro, pco + ((st >> 1) & 1) * 1280, (st >> 2) * 15360); SB(); }
    if constexpr (st == 14) {
#pragma unroll
      for (int i = 0; i < 6; ++i) fma8(y, lds[L_HH + w * 48 + rr + 6 * i], wdb[i]);
      y.pin();
      SB();
    }
    if constexpr (st == 40) {
      MEMBAR();
      r1 = ld16<AUX_SC1>(rws, c * 16, rec); r2 = ld16<AUX_SC1>(rws, prec, 0);
      xo = __builtin_amdgcn_raw_buffer_load_b16(rx, (cg * 16 + 4 * (lane & 3) + row) * 2, 0, AUX_W);
      SB();
    }
    if constexpr (st >= 20 && (st & 3) == 0 && (st - 20) / 4 < 11) {
      constexpr int i = (st - 20) / 4;
#pragma unroll
      for (int k = 0; k < 4; ++k) {
        const unsigned a_ = wob[2 * i][k], b_ = wob[2 * i + 1][k];
        wob[2 * i][k] = __builtin_amdgcn_perm(b_, a_, 0x05040100u);
        wob[2 * i + 1][k] = __builtin_amdgcn_perm(b_, a_, 0x07060302u);
      }
      asm volatile("" : "+v"(wob[2 * i]), "+v"(wob[2 * i + 1]));
      SB();
    }
    if constexpr (st == 58) {
      if (__builtin_expect(__builtin_amdgcn_ballot_w64(((r1[1] ^ tag0) | (r1[3] ^ tag1)) != 0u) != 0ull, 0)) {
        unsigned spin = 0;
        do {
          __builtin_amdgcn_s_sleep(8); MEMBAR();
          r1 = ld16<AUX_SC1>(rws, c * 16, rec);
          __builtin_amdgcn_s_waitcnt(0x0F70);
        } while (__builtin_amdgcn_ballot_w64(((r1[1] ^ tag0) | (r1[3] ^ tag1)) != 0u) != 0ull && ++spin < SPIN_CAP);
      }
      ok2 = __builtin_amdgcn_ballot_w64(((r2[1] ^ tag0) | (r2[3] ^ tag1)) != 0u) == 0ull;
      asm volatile("" :: "v"(r2));
      const float mi = __uint_as_float(r1[0]), li = __uint_as_float(r1[2]);
      const float M = row_max(mi);
      const float L = row_sum(li * __builtin_amdgcn_exp2f((mi - M) * LOG2E));
      fw = __builtin_amdgcn_exp2f((mw - M) * LOG2E) / L;
      asm volatile("" : "+v"(fw));
      SB();
    }
  });
  STAMP(3);
  {
    const float e0 = fold32(acc[0], acc[4]), e1 = fold32(acc[1], acc[5]), e2 = fold32(acc[2], acc[6]), e3 = fold32(acc[3], acc[7]);
    const f32x2 o = {fold16(e0, e2) * fw, fold16(e1, e3) * fw};
    *(f32x2*)(lds + L_OP + w * 128 + 8 * c + 2 * row) = o;
  }
  __syncthreads();
  {
    f32x2 o = {0.f, 0.f};
#pragma unroll
    for (int ww = 0; ww < 8; ++ww) o += *(const f32x2*)(lds + L_OP + ww * 128 + 2 * lane);
    unsigned* ot = (unsigned*)lds + L_OT + w * 72;
    ot[(lane % 6) * 12 + lane / 6] = pk_bf16(o[0], o[1]);
    if (lane < 2) ot[(4 + lane) * 12 + 10] = 0u;
    WAVE_SYNC();
    const unsigned* orow = ot + (rr < 6 ? rr : 0) * 12;
    float yy[8];
#pragma unroll
    for (int k = 0; k < 8; ++k) yy[k] = y[k];
#pragma unroll
    for (int i4 = 0; i4 < 3; ++i4) {
      const u32x4 ov = *(const u32x4*)(orow + 4 * i4);
#pragma unroll
      for (int e = 0; e < 4; ++e) {
        const int i = 4 * i4 + e;
        if (i < 11) {
#pragma unroll
          for (int k = 0; k < 4; ++k) { yy[2 * k] = dot2(wob[2 * i][k], ov[e], yy[2 * k]); yy[2 * k + 1] = dot2(wob[2 * i + 1][k], ov[e], yy[2 * k + 1]); }
        }
      }
    }
    if (tid < 480) {
      *(f32x4*)(lds + L_YP + tid * 8) = f32x4{yy[0], yy[1], yy[2], yy[3]};
      *(f32x4*)(lds + L_YP + tid * 8 + 4) = f32x4{yy[4], yy[5], yy[6], yy[7]};
    }
  }
  __syncthreads();
  if (tid < 160) {
    f32x4 v = {0.f, 0.f, 0.f, 0.f};
#pragma unroll
    for (int i = 0; i < 6; ++i) v += *(const f32x4*)(lds + L_YP + i * 640 + tid * 4);
    u32x4 u = __builtin_bit_cast(u32x4, v);
#pragma unroll
    for (int k = 0; k < 4; ++k) u[k] = u[k] == SENT ? 0x7FC00000u : u[k];
    __builtin_amdgcn_raw_buffer_store_b128(u, rws, tid * 16, slab, AUX_SC1);
  }
  STAMP(4);

  if (red) {
    for (unsigned spin = 0; !ok2 && spin < SPIN_CAP; ++spin) {
      MEMBAR();
      r2 = ld16<AUX_SC1>(rws, prec, 0);
      ok2 = __builtin_amdgcn_ballot_w64(((r2[1] ^ tag0) | (r2[3] ^ tag1)) != 0u) == 0ull;
    }
    const int so = WS_SLAB + b * 64 * DM * 4 + cg * 64, vo = (lane >> 2) * (DM * 4) + (lane & 3) * 16;
    u32x4 pv[4];
#pragma unroll
    for (int i = 0; i < 4; ++i) pv[i] = u32x4{SENT, SENT, SENT, SENT};
    for (unsigned spin = 0; spin < SPIN_CAP; ++spin) {
      MEMBAR();
      bool miss = false;
#pragma unroll
      for (int i = 0; i < 4; ++i) {
        const bool m = pv[i][0] == SENT || pv[i][1] == SENT || pv[i][2] == SENT || pv[i][3] == SENT;
        const u32x4 t = ld16<AUX_SC1>(rws, m ? vo : (1 << 28), so + i * 16 * DM * 4);
        pv[i] = m ? t : pv[i];
      }
#pragma unroll
      for (int i = 0; i < 4; ++i) miss |= pv[i][0] == SENT || pv[i][1] == SENT || pv[i][2] == SENT || pv[i][3] == SENT;
      if (__builtin_amdgcn_ballot_w64(miss) == 0ull) break;
    }
    const f32x4 f = (__builtin_bit_cast(f32x4, pv[0]) + __builtin_bit_cast(f32x4, pv[1])) + (__builtin_bit_cast(f32x4, pv[2]) + __builtin_bit_cast(f32x4, pv[3]));
    const float e0 = fold32(f[0], f[2]), e1 = fold32(f[1], f[3]);
    float v = fold16(e0, e1);
    v += dpp<ROR8>(v); v += dpp<ROR4>(v);
    if (c < 4) out[b * DM + cg * 16 + 4 * c + row] = __builtin_bit_cast(u16, (__bf16)(lo_f(xo) + v));
    if (lane == 0 && cg == 0) __builtin_amdgcn_raw_buffer_store_b32(tag0, rws, WS_EPOCH + b * 256, 0, AUX_SC1);
    STAMP(5);
  }
  STAMP_OUT();
}

}

extern "C" void kernel_launch(void* const* d_in, const int* in_sizes, int n_in, void* d_out, int out_size, void* d_ws, size_t ws_size,
                              hipStream_t stream) {
  (void)in_sizes; (void)n_in; (void)out_size; (void)ws_size;
  fused_decode<<<NWG, NTHR, 0, stream>>>((const u16*)d_in[0], (const u16*)d_in[1], (const u16*)d_in[2], (const u16*)d_in[3],
                                         (const u16*)d_in[4], (const u16*)d_in[6], (const u16*)d_in[7], (const u16*)d_in[8],
                                         (u16*)d_out, (unsigned char*)d_ws);
}
```

```cpp
#include <hip/hip_runtime.h>
#include <hip/hip_bf16.h>
#include <stdint.h>

namespace {

constexpr int NB = 4, DM = 640, NH = 32, SK = 4096, DH = 128, FF = 1728;
constexpr int NWG = 256, NTHR = 512;
#define TAU_NS 274.
#define NE 26
#define CE 1.1
#define SLACK0 1.
#define SLACKM 0.5
#define NODRAIN 0
#define L2W 1
#define NOVALU 0
#define ABSPH 0
#define WARM 1
#define RING 8
#define XP 13
#define PRE 22
#define RLD 38
#define AUXW 0
#define WCOST 0.
#define SKEW 25
#define NOW 0
#define RPH 0
#define CATCH 4
#define PHASE_WG 1
#define MINGAP 0.

typedef unsigned short u16;
typedef unsigned u32x4 __attribute__((ext_vector_type(4)));
typedef unsigned u32x2 __attribute__((ext_vector_type(2)));
typedef float f32x4 __attribute__((ext_vector_type(4)));
typedef float f32x2 __attribute__((ext_vector_type(2)));
typedef __bf16 bf16x2 __attribute__((ext_vector_type(2)));
typedef __amdgpu_buffer_rsrc_t rsrc_t;

constexpr int AUX_K = 2, AUX_V = 2, AUX_W = AUXW, AUX_NT = 2, AUX_SC1 = 16, RSRC_W3 = 0x00020000;

constexpr int WS_EPOCH = 0, WS_FLAG = 4096, WS_REC = 8192, WS_SLAB = WS_REC + NB * NH * 16 * 16, WS_X = WS_SLAB + NB * 64 * DM * 4, WS_END = WS_X + NH * NB * 8192;
constexpr int WS_STAMP = 4 << 20;
constexpr unsigned SENT = 0xFFFFFFFFu;
constexpr unsigned SPIN_CAP = 1u << 18;
constexpr int OOB = 1 << 28;

constexpr int L_XN = 0;
constexpr int L_PP = L_XN + 640 * 4;
constexpr int L_QP = L_PP + 8 * 256;
constexpr int L_HH = L_QP + 8 * 128;
constexpr int L_OP = L_HH + 8 * 48;
constexpr int L_OT = L_OP + 8 * 128;
constexpr int L_YP = L_OT + 8 * 72;
constexpr int L_END = L_YP + 6 * 640;

__device__ __forceinline__ rsrc_t mk(const void* p, unsigned bytes) {
  return __builtin_amdgcn_make_buffer_rsrc(const_cast<void*>(p), 0, (int)bytes, RSRC_W3);
}
template <int AUX> __device__ __forceinline__ u32x4 ld16(rsrc_t r, int voff, int soff) {
  return __builtin_amdgcn_raw_buffer_load_b128(r, voff, soff, AUX);
}
__device__ __forceinline__ float lo_f(unsigned u) { return __builtin_bit_cast(float, u << 16); }
__device__ __forceinline__ float hi_f(unsigned u) { return __builtin_bit_cast(float, u & 0xffff0000u); }
__device__ __forceinline__ float bfr(float f) { return (float)(__bf16)f; }
__device__ __forceinline__ unsigned pk_bf16(float lo, float hi) {
  bf16x2 v = {(__bf16)lo, (__bf16)hi};
  return __builtin_bit_cast(unsigned, v);
}
__device__ __forceinline__ float dot2(unsigned a, unsigned b, float acc) {
  return __builtin_amdgcn_fdot2_f32_bf16(__builtin_bit_cast(bf16x2, a), __builtin_bit_cast(bf16x2, b), acc, false);
}
template <int CTRL> __device__ __forceinline__ float dpp(float x) {
  return __builtin_bit_cast(float, __builtin_amdgcn_mov_dpp(__builtin_bit_cast(int, x), CTRL, 0xf, 0xf, true));
}
constexpr int XOR1 = 0xB1, XOR2 = 0x4E, XOR7 = 0x141, ROR8 = 0x128, ROR4 = 0x124, BCAST = 0x150;
__device__ __forceinline__ float fold32(float a, float b) {
  auto s = __builtin_amdgcn_permlane32_swap(__builtin_bit_cast(unsigned, a), __builtin_bit_cast(unsigned, b), false, false);
  return __builtin_bit_cast(float, (unsigned)s[0]) + __builtin_bit_cast(float, (unsigned)s[1]);
}
__device__ __forceinline__ float fold16(float a, float b) {
  auto s = __builtin_amdgcn_permlane16_swap(__builtin_bit_cast(unsigned, a), __builtin_bit_cast(unsigned, b), false, false);
  return __builtin_bit_cast(float, (unsigned)s[0]) + __builtin_bit_cast(float, (unsigned)s[1]);
}
__device__ __forceinline__ float row_sum(float v) {
  v += dpp<XOR1>(v); v += dpp<XOR2>(v); v += dpp<XOR7>(v); v += dpp<ROR8>(v); return v;
}
__device__ __forceinline__ float row_max(float v) {
  v = fmaxf(v, dpp<XOR1>(v)); v = fmaxf(v, dpp<XOR2>(v)); v = fmaxf(v, dpp<XOR7>(v)); v = fmaxf(v, dpp<ROR8>(v)); return v;
}
__device__ __forceinline__ float wave_sum(float v) { v = row_sum(v); v = fold16(v, v); return fold32(v, v); }
__device__ __forceinline__ float wave_max(float v) {
  v = row_max(v);
  auto s = __builtin_amdgcn_permlane16_swap(__builtin_bit_cast(unsigned, v), __builtin_bit_cast(unsigned, v), false, false);
  v = fmaxf(__builtin_bit_cast(float, (unsigned)s[0]), __builtin_bit_cast(float, (unsigned)s[1]));
  auto t = __builtin_amdgcn_permlane32_swap(__builtin_bit_cast(unsigned, v), __builtin_bit_cast(unsigned, v), false, false);
  return fmaxf(__builtin_bit_cast(float, (unsigned)t[0]), __builtin_bit_cast(float, (unsigned)t[1]));
}
struct Acc8 {
  f32x2 v[4];
  __device__ __forceinline__ void zero() { v[0] = v[1] = v[2] = v[3] = f32x2{0.f, 0.f}; }
  __device__ __forceinline__ float operator[](int i) const { return v[i >> 1][i & 1]; }
  __device__ __forceinline__ void pin() { asm volatile("" : "+v"(v[0]), "+v"(v[1]), "+v"(v[2]), "+v"(v[3])); }
};
__device__ __forceinline__ void fma8(Acc8& acc, float s, u32x4 v) {
  const f32x2 ss = {s, s};
#pragma unroll
  for (int k = 0; k < 4; ++k) acc.v[k] = __builtin_elementwise_fma(ss, f32x2{lo_f(v[k]), hi_f(v[k])}, acc.v[k]);
}
#define MEMBAR() asm volatile("" ::: "memory")
#define SB() __builtin_amdgcn_sched_barrier(0)
#define WAVE_SYNC() do { __builtin_amdgcn_fence(__ATOMIC_RELEASE, "wavefront"); __builtin_amdgcn_wave_barrier(); __builtin_amdgcn_fence(__ATOMIC_ACQUIRE, "wavefront"); } while (0)

#define STAMP(i) do {} while (0)
#define STAMP_OUT() do {} while (0)

#define ESTAMP(i) do {} while (0)
#define LSTAMP(i) STAMP(i)
template <int I> struct IC { static constexpr int value = I; };
template <int I, int N, class F> __device__ __forceinline__ void static_for(F&& f) {
  if constexpr (I < N) { f(IC<I>{}); static_for<I + 1, N>(f); }
}
__device__ __forceinline__ float transpose_reduce(float (&x)[16], int c) {
#define TR_STEP(HALF, CTRL) _Pragma("unroll") for (int i = 0; i < (HALF); ++i) { \
    const float lo_ = x[i], hi_ = x[i + (HALF)]; const float t1_ = lo_ + dpp<CTRL>(lo_), t2_ = hi_ + dpp<CTRL>(hi_); \
    x[i] = (c & (HALF)) ? t2_ : t1_; }
  TR_STEP(8, ROR8) TR_STEP(4, XOR7) TR_STEP(2, XOR2) TR_STEP(1, XOR1)
#undef TR_STEP
  return x[0];
}

constexpr int D0 = PRE + 2, O0 = D0 + 18;
static_assert(O0 + 3 * 21 + 2 < 124, "wo lands before its use");
constexpr int slot_at(int n, int first, int cnt) { return (n >= first && (n - first) % 3 == 0 && (n - first) / 3 < cnt) ? (n - first) / 3 : -1; }
constexpr double cost_before(int n) {
  double t = 0;
  for (int m = -5; m < n; ++m) {
    t += m < NE ? CE : 1.;
    t += (slot_at(m, D0, 6) >= 0 ? .176 * WCOST : 0.) + (slot_at(m, O0, 22) >= 0 ? .114 * WCOST : 0.) + (m == 0 ? .3 : 0.) + (m == 6 ? .6 : 0.) + (m == XP ? CE : 0.);
    t += L2W ? (m == 8 ? 2.5 * CE : 0.) + (m == 16 ? 1.05 * CE : 0.) : 0.;
    t += (m == NE - 1 ? SLACK0 : 0.) + (m == 64 + RING + 1 ? SLACKM : 0.);
  }
  return t;
}
constexpr double TAU = TAU_NS / 10.;
static_assert((XP & 1) && !(PRE & 1) && !(RING & 1), "");
constexpr bool gated(int n) { return TAU_NS > 0 && ((n & 1) != 0) == (n <= XP); }
constexpr int target(int u) {
  const int drip = PRE + 2 * ((u + 1 + 2 * CATCH - 1) / (2 * CATCH)), mid = u >= 64 ? 64 + RING + 2 : 0;
  int t = (u + 1 + RING) & ~1;
  t = t < drip ? drip : t; t = t < mid ? mid : t;
  return u < 0 ? PRE : (t > 128 ? 128 : t);
}
template <int DUE> __device__ __forceinline__ void gate(unsigned base, unsigned& last) {
  if constexpr (MINGAP > 0) {
    unsigned due = max(last + (unsigned)(MINGAP * 2 * TAU), (unsigned)DUE);
    asm volatile("1:\n\ts_memrealtime vcc\n\ts_waitcnt lgkmcnt(0)\n\ts_sub_u32 %0, vcc_lo, %1\n\ts_cmp_ge_i32 %0, %2\n\ts_cbranch_scc1 2f\n\ts_sleep 1\n\ts_branch 1b\n2:"
                 : "=&s"(last) : "s"(base), "s"(due) : "vcc", "scc");
  } else {
    asm volatile("1:\n\ts_memrealtime vcc\n\ts_waitcnt lgkmcnt(0)\n\ts_sub_u32 vcc_lo, vcc_lo, %0\n\ts_cmp_ge_i32 vcc_lo, %1\n\ts_cbranch_scc1 2f\n\ts_sleep 1\n\ts_branch 1b\n2:"
                 :: "s"(base), "n"(DUE) : "vcc", "scc");
  }
}

__global__ void __launch_bounds__(NTHR, 2)
fused_decode(const u16* x, const u16* __restrict__ kc, const u16* __restrict__ vc, const u16* __restrict__ wq,
             const u16* __restrict__ wo, const u16* __restrict__ wg, const u16* __restrict__ wu, const u16* __restrict__ wd,
             u16* __restrict__ out, unsigned char* ws) {
  __shared__ __attribute__((aligned(16))) float lds[L_END];
  const int tid = threadIdx.x, lane = tid & 63, w = __builtin_amdgcn_readfirstlane(tid >> 6);
  const int bid = blockIdx.x, j = bid >> 3;
  const int h = (bid & 7) * 4 + (j >> 3), b = (j & 7) >> 1, s = j & 1;
  const int pidx = 2 * h + s;
  const int row = lane >> 4, c = lane & 15;
  STAMP(0);
  const size_t kvoff = ((size_t)(b * NH + h) * SK + s * 2048 + w * 256) * DH;
  const rsrc_t rk = mk(kc + kvoff, 65536), rv = mk(vc + kvoff, 65536);
  const unsigned rid = (unsigned)(((b * NH + h) * 2 + s) * 8 + w);
  const unsigned rot = (rid ^ (rid >> 6)) & 63u;
  int voffs[4];
#pragma unroll
  for (int k = 0; k < 4; ++k) voffs[k] = lane * 16 + (int)((k ^ rot) & 3u) * 1024;
  const int rhi = (int)(rot >> 2) * 4096;
  const int fb = (j >> 3) * 8 + (bid & 7);
  const bool ffn = fb < FF / 64;
  const int pc = ffn ? fb : 0;
  const rsrc_t ro = mk(wo + (size_t)h * DH * DM, NOW ? 0 : DH * DM * 2);
  const rsrc_t rd = mk(wd + (size_t)(2 * pc + s) * 32 * DM, (ffn && !NOW) ? 32 * DM * 2 : 0);
  const int rr = tid / 80;
  const int pcs = tid < 480 ? tid * 16 : OOB;
  const int pco = tid < 480 ? tid * 16 + rr * 1280 : OOB;
  const rsrc_t rws = mk(ws, WS_END);
  const int xoff = WS_X + ((h * NB + b) * 8 + w) * 1024;
  const bool xuse = ffn || w < 4;
  const int krow = 160 * b + 40 * (w & 3) + (lane >> 3);
  const int pitch = w < 4 ? NH * DH * 2 : FF * 2, col = w < 4 ? h * DH + s * 64 : pc * 64;
  const rsrc_t rw = mk((w < 4 ? wq : s ? wu : wg) + col, xuse ? (unsigned)(DM * pitch - col * 2) : 0u);
  const int vw = krow * pitch + (lane & 7) * 16;
  unsigned xr[5];
  {
    const rsrc_t rxw = mk(x + (w & 3) * DM, w < 4 ? DM * 2 : 0);
#pragma unroll
    for (int i = 0; i < 5; ++i) xr[i] = __builtin_amdgcn_raw_buffer_load_b32(rxw, lane * 4 + i * 256, 0, AUX_W);
  }
  const unsigned epoch = __builtin_amdgcn_raw_buffer_load_b32(rws, WS_EPOCH, 0, AUX_SC1);
  unsigned cw[3];
  {
    unsigned long long pc; unsigned len;
    asm volatile("s_getpc_b64 %0\n.Lfd_code0:\n\ts_mov_b32 %1, .Lfd_code1-.Lfd_code0" : "=s"(pc), "=s"(len));
    const rsrc_t rc = mk((const void*)pc, (j & 15) == 15 ? len : 0u);
#pragma unroll
    for (int i = 0; i < 3; ++i) cw[i] = __builtin_amdgcn_raw_buffer_load_b32(rc, w == 5 ? lane * 128 : OOB, i * 8192, 0);
  }
  MEMBAR();
  SB();
  const unsigned t0 = (unsigned)__builtin_amdgcn_s_memrealtime();
  constexpr unsigned GI = (unsigned)(2 * TAU + .5);
  const unsigned phi = ((RPH ? ((unsigned)bid * 0x9E3779B1u) >> 24 : (unsigned)bid) * GI) >> 8;
  const unsigned tbase = __builtin_amdgcn_readfirstlane(ABSPH ? t0 + (phi + GI - t0 % GI) % GI : t0 + phi);
  unsigned tlast = 0;
  u32x4 kv[128], wb[5], wdb[6], wob[22], rx4;
  unsigned lw0 = 0, lw1 = 0;
  auto issue = [&](auto I) {
    constexpr int n = decltype(I)::value;
    if constexpr (gated(n)) {
      gate<(int)(TAU * cost_before(n))>(tbase, tlast);
    }
    if constexpr (n < 0) wb[n + 5] = ld16<AUX_NT>(rw, vw, (n + 5) * 8 * pitch);
    else if constexpr (n < 64) kv[n] = ld16<AUX_K>(rk, voffs[n & 3], rhi ^ ((n >> 2) * 4096));
    else kv[n] = ld16<AUX_V>(rv, voffs[n & 3], rhi ^ (((n & 63) >> 2) * 4096));
    SB();
    constexpr int id = slot_at(n, D0, 6), io = slot_at(n, O0, 22);
    if constexpr (id >= 0) wdb[id] = ld16<AUX_W>(rd, pcs, id * 7680);
    if constexpr (io >= 0) wob[io] = ld16<AUX_W>(ro, pco + (io & 1) * 1280, (io >> 1) * 15360);
    if constexpr (L2W && n == 8) lw0 = __builtin_amdgcn_raw_buffer_load_b32(ro, lane < 20 ? lane * 128 : OOB, ((j & 7) * 8 + w) * 2560, AUX_W);
    if constexpr (L2W && n == 16) lw1 = __builtin_amdgcn_raw_buffer_load_b32(rd, lane < 10 ? lane * 128 : OOB, (b * 8 + w) * 1280, AUX_W);
    if constexpr (n == XP) { MEMBAR(); rx4 = ld16<AUX_SC1>(rws, xuse ? lane * 16 : OOB, xoff); }
    SB();
  };
  static_for<-5, -1>(issue);
  STAMP(6);
  if (w < 4) {
    float xv[10], sum = 0.f;
#pragma unroll
    for (int i = 0; i < 5; ++i) { xv[2 * i] = lo_f(xr[i]); xv[2 * i + 1] = hi_f(xr[i]); sum += xv[2 * i] + xv[2 * i + 1]; }
    const float mean = wave_sum(sum) * (1.f / DM);
    float sq = 0.f;
#pragma unroll
    for (int i = 0; i < 10; ++i) { const float d = xv[i] - mean; sq = fmaf(d, d, sq); }
    const float var = bfr(wave_sum(sq) * (1.f / DM));
    const float mb = bfr(mean), rs = 1.f / bfr(sqrtf(bfr(var + 1e-5f)));
#pragma unroll
    for (int i = 0; i < 10; ++i) lds[L_XN + (2 * lane + 128 * (i >> 1) + (i & 1)) * 4 + w] = bfr(bfr(xv[i] - mb) * rs);
  }
  SB();
  static_for<-1, 1>(issue);
  const int slab = WS_SLAB + (b * 64 + pidx) * (DM * 4);
  if (w == 7) {
    const u32x4 sv = {SENT, SENT, SENT, SENT};
    __builtin_amdgcn_raw_buffer_store_b128(sv, rws, lane * 16, slab, AUX_SC1);
    __builtin_amdgcn_raw_buffer_store_b128(sv, rws, lane * 16 + 1024, slab, AUX_SC1);
    __builtin_amdgcn_raw_buffer_store_b128(sv, rws, lane < 32 ? lane * 16 + 2048 : OOB, slab, AUX_SC1);
  }
  asm volatile("" :: "v"(cw[0]), "v"(cw[1]), "v"(cw[2]));
  ESTAMP(2);
  __syncthreads();
  ESTAMP(3);
  const unsigned tag0 = epoch + 1u, tag1 = (epoch + 1u) * 0x9E3779B1u + 0x7F4A7C15u;
  {
    Acc8 a[4];
#pragma unroll
    for (int bb = 0; bb < 4; ++bb) a[bb].zero();
    static_for<0, 5>([&](auto T) {
      constexpr int t = decltype(T)::value;
      const f32x4 xn4 = *(const f32x4*)(lds + L_XN + (krow + 8 * t) * 4);
#pragma unroll
      for (int bb = 0; bb < 4; ++bb) { fma8(a[bb], xn4[bb], wb[t]); a[bb].pin(); }
      SB();
      if constexpr (t == 1) static_for<1, 3>(issue);
      if constexpr (t == 4) static_for<3, 5>(issue);
    });
    float v[8];
#pragma unroll
    for (int k = 0; k < 8; ++k) { v[k] = fold16(fold32(a[0][k], a[2][k]), fold32(a[1][k], a[3][k])); v[k] += dpp<ROR8>(v[k]); }
    const bool up = lane & 8;
    *(f32x4*)(lds + L_PP + w * 256 + row * 64 + (lane & 7) * 8 + (up ? 4 : 0)) = f32x4{up ? v[4] : v[0], up ? v[5] : v[1], up ? v[6] : v[2], up ? v[7] : v[3]};
  }
  SB();
  static_for<5, 7>(issue);
  ESTAMP(4);
  __syncthreads();
  if (w < 4) {
    f32x2 v = {0.f, 0.f};
#pragma unroll
    for (int i = 0; i < 4; ++i) v += *(const f32x2*)(lds + L_PP + ((tid >> 7) * 4 + i) * 256 + 2 * (tid & 127));
    const u32x4 r = {__float_as_uint(v[0]), tag0, __float_as_uint(v[1]), tag1};
    const int o = (((tid >> 5) & 3) * 8 + (tid >> 7) * 4 + b) * 1024 + s * 512 + (tid & 31) * 16;
    __builtin_amdgcn_raw_buffer_store_b128(r, rws, (ffn || w < 2) ? o : OOB, WS_X + h * NB * 8192, AUX_SC1);
  }
  SB();
  STAMP(7);
  static_for<7, PRE - 2>(issue);
  if (__builtin_expect(xuse && __builtin_amdgcn_ballot_w64(((rx4[1] ^ tag0) | (rx4[3] ^ tag1)) != 0u) != 0ull, 0)) {
    unsigned spin = 0;
    do {
      MEMBAR();
      rx4 = ld16<AUX_SC1>(rws, lane * 16, xoff);
      __builtin_amdgcn_s_waitcnt(0x0F70);
    } while (__builtin_amdgcn_ballot_w64(((rx4[1] ^ tag0) | (rx4[3] ^ tag1)) != 0u) != 0ull && ++spin < SPIN_CAP);
  }
  *(f32x2*)(lds + L_QP + w * 128 + 2 * lane) = f32x2{__uint_as_float(rx4[0]), __uint_as_float(rx4[2])};
  SB();
  static_for<PRE - 2, PRE>(issue);
  __syncthreads();
  u32x4 q;
  {
    f32x4 s0 = {0.f, 0.f, 0.f, 0.f}, s1 = {0.f, 0.f, 0.f, 0.f};
    float gu = 0.f;
#pragma unroll
    for (int kq = 0; kq < 4; ++kq) {
      s0 += *(const f32x4*)(lds + L_QP + kq * 128 + 8 * c); s1 += *(const f32x4*)(lds + L_QP + kq * 128 + 8 * c + 4);
      gu += lds[L_QP + (4 + kq) * 128 + (lane >> 5) * 64 + 32 * s + (lane & 31)];
    }
    q[0] = pk_bf16(s0[0], s0[1]); q[1] = pk_bf16(s0[2], s0[3]); q[2] = pk_bf16(s1[0], s1[1]); q[3] = pk_bf16(s1[2], s1[3]);
    gu = bfr(gu);
    auto sw = __builtin_amdgcn_permlane32_swap(__float_as_uint(gu), __float_as_uint(gu), false, false);
    const float hv = bfr(__uint_as_float(sw[0]) * __uint_as_float(sw[1]));
    if (lane < 48) lds[L_HH + w * 48 + lane] = lane < 32 ? hv : 0.f;
    WAVE_SYNC();
  }
  STAMP(1);

  Acc8 y; y.zero();
  auto side = [&](auto U) {
    constexpr int u = decltype(U)::value;
    if constexpr (L2W && u == 18) asm volatile("" :: "v"(lw0), "v"(lw1));
    if constexpr (u >= 20 && u < 84 && (u & 7) == 4) {
      if (w == 7 && (j & 7) == (u - 20) / 8) {
        if (!NODRAIN) asm volatile("s_waitcnt vmcnt(0)" ::: "memory");
        __builtin_amdgcn_raw_buffer_store_b128(u32x4{0u, tag0, 0u, tag1}, rws, lane == 0 ? 0 : OOB, WS_FLAG + (b * 64 + pidx) * 16, AUX_SC1);
      }
      SB();
    }
    if constexpr (slot_at(u - 1, D0, 6) >= 0) {
      constexpr int i = slot_at(u - 1, D0, 6);
      fma8(y, lds[L_HH + w * 48 + rr + 6 * i], wdb[i]);
      y.pin();
      SB();
    }
    if constexpr (u >= O0 + 4 && (u - O0 - 4) % 6 == 0 && (u - O0 - 4) / 6 < 11) {
      constexpr int i = (u - O0 - 4) / 6;
#pragma unroll
      for (int k = 0; k < 4; ++k) {
        const unsigned a_ = wob[2 * i][k], b_ = wob[2 * i + 1][k];
        wob[2 * i][k] = __builtin_amdgcn_perm(b_, a_, 0x05040100u);
        wob[2 * i + 1][k] = __builtin_amdgcn_perm(b_, a_, 0x07060302u);
      }
      asm volatile("" : "+v"(wob[2 * i]), "+v"(wob[2 * i + 1]));
      SB();
    }
  };

  constexpr float SCALE = 0.08838834764831845f, LOG2E = 1.4426950408889634f;
  float sc[4];
  {
    float xx[16];
    static_for<0, 64>([&](auto I) {
      constexpr int st = decltype(I)::value;
      float d = dot2(kv[st][0], q[0], 0.f);
      d = dot2(kv[st][1], q[1], d); d = dot2(kv[st][2], q[2], d); d = dot2(kv[st][3], q[3], d);
      asm volatile("" : "+v"(d));
      xx[st & 15] = d;
      SB();
      static_for<target(st - 1), target(st)>(issue);
      side(I);
      if constexpr ((st & 15) == 15) { sc[st >> 4] = bfr(bfr(transpose_reduce(xx, c)) * SCALE); SB(); }
    });
  }
  LSTAMP(2);
  float mw = wave_max(fmaxf(fmaxf(sc[0], sc[1]), fmaxf(sc[2], sc[3])));
  asm volatile("" : "+v"(mw));
  SB();
  static_for<target(63), target(64)>(issue);
  float p[4];
#pragma unroll
  for (int g = 0; g < 4; ++g) p[g] = __builtin_amdgcn_exp2f((sc[g] - mw) * LOG2E);
  const float lw = wave_sum((p[0] + p[1]) + (p[2] + p[3]));
  const int rec = WS_REC + (b * NH + h) * 256;
  {
    const u32x4 r = {__float_as_uint(mw), tag0, __float_as_uint(lw), tag1};
    __builtin_amdgcn_raw_buffer_store_b128(r, rws, lane == 0 ? (s * 8 + w) * 16 : OOB, rec, AUX_SC1);
  }
  SB();

  Acc8 acc; acc.zero();
  const int cg = pidx - 24;
  const bool red = w == 7 && cg >= 0;
  const int prec = red ? WS_FLAG + (b * 64 + lane) * 16 : OOB;
  u32x4 r1, r2;
  unsigned xo = 0;
  float fw = 0.f;
  bool ok2 = false;
  static_for<0, 64>([&](auto I) {
    constexpr int st = decltype(I)::value;
    if (NOVALU) { acc.v[0][0] += __uint_as_float(kv[64 + st][0] ^ kv[64 + st][1] ^ kv[64 + st][2] ^ kv[64 + st][3]); } else
    fma8(acc, dpp<BCAST + (st & 15)>(p[st >> 4]), kv[64 + st]);
    acc.pin();
    SB();
    if constexpr (st > 0) static_for<target(63 + st), target(64 + st)>(issue);
    side(IC<64 + st>{});
    if constexpr (st == RLD) { MEMBAR(); r1 = ld16<AUX_SC1>(rws, c * 16, rec); SB(); }
    if constexpr (st == RLD + 2) { MEMBAR(); r2 = ld16<AUX_SC1>(rws, prec, 0); SB(); }
    if constexpr (st == RLD + 4) { xo = __builtin_amdgcn_raw_buffer_load_b16(mk(x + b * DM, red ? DM * 2 : 0), (cg * 16 + 4 * (lane & 3) + row) * 2, 0, AUX_W); SB(); }
    if constexpr (st == RLD + RING + 4) {
      if (__builtin_expect(__builtin_amdgcn_ballot_w64(((r1[1] ^ tag0) | (r1[3] ^ tag1)) != 0u) != 0ull, 0)) {
        unsigned spin = 0;
        do {
          __builtin_amdgcn_s_sleep(8); MEMBAR();
          r1 = ld16<AUX_SC1>(rws, c * 16, rec);
          __builtin_amdgcn_s_waitcnt(0x0F70);
        } while (__builtin_amdgcn_ballot_w64(((r1[1] ^ tag0) | (r1[3] ^ tag1)) != 0u) != 0ull && ++spin < SPIN_CAP);
      }
      ok2 = __builtin_amdgcn_ballot_w64(((r2[1] ^ tag0) | (r2[3] ^ tag1)) != 0u) == 0ull;
      asm volatile("" :: "v"(r2));
      const float mi = __uint_as_float(r1[0]), li = __uint_as_float(r1[2]);
      const float M = row_max(mi);
      const float L = row_sum(li * __builtin_amdgcn_exp2f((mi - M) * LOG2E));
      fw = __builtin_amdgcn_exp2f((mw - M) * LOG2E) / L;
      asm volatile("" : "+v"(fw));
      SB();
    }
  });
  LSTAMP(3);
  {
    const float e0 = fold32(acc[0], acc[4]), e1 = fold32(acc[1], acc[5]), e2 = fold32(acc[2], acc[6]), e3 = fold32(acc[3], acc[7]);
    const f32x2 o = {fold16(e0, e2) * fw, fold16(e1, e3) * fw};
    *(f32x2*)(lds + L_OP + w * 128 + 8 * c + 2 * row) = o;
  }
  __syncthreads();
  {
    f32x2 o = {0.f, 0.f};
#pragma unroll
    for (int ww = 0; ww < 8; ++ww) o += *(const f32x2*)(lds + L_OP + ww * 128 + 2 * lane);
    unsigned* ot = (unsigned*)lds + L_OT + w * 72;
    ot[(lane % 6) * 12 + lane / 6] = pk_bf16(o[0], o[1]);
    if (lane < 2) ot[(4 + lane) * 12 + 10] = 0u;
    WAVE_SYNC();
    const unsigned* orow = ot + (rr < 6 ? rr : 0) * 12;
    float yy[8];
#pragma unroll
    for (int k = 0; k < 8; ++k) yy[k] = y[k];
#pragma unroll
    for (int i4 = 0; i4 < 3; ++i4) {
      const u32x4 ov = *(const u32x4*)(orow + 4 * i4);
#pragma unroll
      for (int e = 0; e < 4; ++e) {
        const int i = 4 * i4 + e;
        if (i < 11) {
#pragma unroll
          for (int k = 0; k < 4; ++k) { yy[2 * k] = dot2(wob[2 * i][k], ov[e], yy[2 * k]); yy[2 * k + 1] = dot2(wob[2 * i + 1][k], ov[e], yy[2 * k + 1]); }
        }
      }
    }
    if (tid < 480) {
      *(f32x4*)(lds + L_YP + tid * 8) = f32x4{yy[0], yy[1], yy[2], yy[3]};
      *(f32x4*)(lds + L_YP + tid * 8 + 4) = f32x4{yy[4], yy[5], yy[6], yy[7]};
    }
  }
  __syncthreads();
  if (tid < 160) {
    f32x4 v = {0.f, 0.f, 0.f, 0.f};
#pragma unroll
    for (int i = 0; i < 6; ++i) v += *(const f32x4*)(lds + L_YP + i * 640 + tid * 4);
    u32x4 u = __builtin_bit_cast(u32x4, v);
#pragma unroll
    for (int k = 0; k < 4; ++k) u[k] = u[k] == SENT ? 0x7FC00000u : u[k];
    __builtin_amdgcn_raw_buffer_store_b128(u, rws, tid * 16, slab, AUX_SC1);
  }
  LSTAMP(4);

  if (red) {
    for (unsigned spin = 0; !ok2 && spin < SPIN_CAP; ++spin) {
      MEMBAR();
      r2 = ld16<AUX_SC1>(rws, prec, 0);
      ok2 = __builtin_amdgcn_ballot_w64(((r2[1] ^ tag0) | (r2[3] ^ tag1)) != 0u) == 0ull;
    }
    const int so = WS_SLAB + b * 64 * DM * 4 + cg * 64, vo = (lane >> 2) * (DM * 4) + (lane & 3) * 16;
    u32x4 pv[4];
#pragma unroll
    for (int i = 0; i < 4; ++i) pv[i] = u32x4{SENT, SENT, SENT, SENT};
    for (unsigned spin = 0; spin < SPIN_CAP; ++spin) {
      MEMBAR();
      bool miss = false;
#pragma unroll
      for (int i = 0; i < 4; ++i) {
        const bool m = pv[i][0] == SENT || pv[i][1] == SENT || pv[i][2] == SENT || pv[i][3] == SENT;
        const u32x4 t = ld16<AUX_SC1>(rws, m ? vo : OOB, so + i * 16 * DM * 4);
        pv[i] = m ? t : pv[i];
      }
#pragma unroll
      for (int i = 0; i < 4; ++i) miss |= pv[i][0] == SENT || pv[i][1] == SENT || pv[i][2] == SENT || pv[i][3] == SENT;
      if (__builtin_amdgcn_ballot_w64(miss) == 0ull) break;
    }
    const f32x4 f = (__builtin_bit_cast(f32x4, pv[0]) + __builtin_bit_cast(f32x4, pv[1])) + (__builtin_bit_cast(f32x4, pv[2]) + __builtin_bit_cast(f32x4, pv[3]));
    const float e0 = fold32(f[0], f[2]), e1 = fold32(f[1], f[3]);
    float v = fold16(e0, e1);
    v += dpp<ROR8>(v); v += dpp<ROR4>(v);
    __builtin_amdgcn_raw_buffer_store_b16(__builtin_bit_cast(u16, (__bf16)(lo_f(xo) + v)), mk(out, NB * DM * 2), c < 4 ? (b * DM + cg * 16 + 4 * c + row) * 2 : OOB, 0, 0);
    if (lane == 0 && cg == 0 && b == 0) __builtin_amdgcn_raw_buffer_store_b32(tag0, rws, WS_EPOCH, 0, AUX_SC1);
    STAMP(5);
  }
  STAMP_OUT();
  asm volatile(".Lfd_code1:");
}

}

extern "C" void kernel_launch(void* const* d_in, const int* in_sizes, int n_in, void* d_out, int out_size, void* d_ws, size_t ws_size,
                              hipStream_t stream) {
  (void)in_sizes; (void)n_in; (void)out_size; (void)ws_size;
  fused_decode<<<NWG, NTHR, 0, stream>>>((const u16*)d_in[0], (const u16*)d_in[1], (const u16*)d_in[2], (const u16*)d_in[3],
                                         (const u16*)d_in[4], (const u16*)d_in[6], (const u16*)d_in[7], (const u16*)d_in[8],
                                         (u16*)d_out, (unsigned char*)d_ws);
}
```

```cpp
#include <hip/hip_runtime.h>
#include <hip/hip_bf16.h>
#include <stdint.h>

namespace {

constexpr int NB = 4, DM = 640, NH = 32, SK = 4096, DH = 128, FF = 1728;
constexpr int NWG = 256, NTHR = 512;
#define TAU_NS 272.
#define CW 1.15
#define CL 1.15
#define CP .92
#define CX 1.15
#define SLACK0 1.
#define SLACKM 0.25
#define RING 8
#define RLD 40
#define CATCH 4
#define NPOLL 3
#define AUXW 0

typedef unsigned short u16;
typedef unsigned u32x4 __attribute__((ext_vector_type(4)));
typedef unsigned u32x2 __attribute__((ext_vector_type(2)));
typedef float f32x4 __attribute__((ext_vector_type(4)));
typedef float f32x2 __attribute__((ext_vector_type(2)));
typedef __bf16 bf16x2 __attribute__((ext_vector_type(2)));
typedef __amdgpu_buffer_rsrc_t rsrc_t;

constexpr int AUX_K = 2, AUX_V = 2, AUX_W = AUXW, AUX_NT = 2, AUX_SC1 = 16, RSRC_W3 = 0x00020000;

constexpr int WS_EPOCH = 0, WS_FLAG = 4096, WS_REC = 8192, WS_SLAB = WS_REC + NB * NH * 16 * 16, WS_X = WS_SLAB + NB * 64 * DM * 4, WS_END = WS_X + NH * NB * 8192;
constexpr int WS_STAMP = 4 << 20;
constexpr unsigned SENT = 0xFFFFFFFFu;
constexpr unsigned SPIN_CAP = 1u << 18;
constexpr int OOB = 1 << 28;

constexpr int L_XN = 0;
constexpr int L_PP = L_XN + 640 * 4;
constexpr int L_QP = L_PP + 8 * 256;
constexpr int L_HH = L_QP + 8 * 128;
constexpr int L_OP = L_HH + 8 * 48;
constexpr int L_OT = L_OP + 8 * 128;
constexpr int L_YP = L_OT + 8 * 72;
constexpr int L_END = L_YP + 6 * 640;

__device__ __forceinline__ rsrc_t mk(const void* p, unsigned bytes) {
  return __builtin_amdgcn_make_buffer_rsrc(const_cast<void*>(p), 0, (int)bytes, RSRC_W3);
}
template <int AUX> __device__ __forceinline__ u32x4 ld16(rsrc_t r, int voff, int soff) {
  return __builtin_amdgcn_raw_buffer_load_b128(r, voff, soff, AUX);
}
__device__ __forceinline__ float lo_f(unsigned u) { return __builtin_bit_cast(float, u << 16); }
__device__ __forceinline__ float hi_f(unsigned u) { return __builtin_bit_cast(float, u & 0xffff0000u); }
__device__ __forceinline__ float bfr(float f) { return (float)(__bf16)f; }
__device__ __forceinline__ unsigned pk_bf16(float lo, float hi) {
  bf16x2 v = {(__bf16)lo, (__bf16)hi};
  return __builtin_bit_cast(unsigned, v);
}
__device__ __forceinline__ float dot2(unsigned a, unsigned b, float acc) {
  return __builtin_amdgcn_fdot2_f32_bf16(__builtin_bit_cast(bf16x2, a), __builtin_bit_cast(bf16x2, b), acc, false);
}
template <int CTRL> __device__ __forceinline__ float dpp(float x) {
  return __builtin_bit_cast(float, __builtin_amdgcn_mov_dpp(__builtin_bit_cast(int, x), CTRL, 0xf, 0xf, true));
}
constexpr int XOR1 = 0xB1, XOR2 = 0x4E, XOR7 = 0x141, ROR8 = 0x128, ROR4 = 0x124, BCAST = 0x150;
__device__ __forceinline__ float fold32(float a, float b) {
  auto s = __builtin_amdgcn_permlane32_swap(__builtin_bit_cast(unsigned, a), __builtin_bit_cast(unsigned, b), false, false);
  return __builtin_bit_cast(float, (unsigned)s[0]) + __builtin_bit_cast(float, (unsigned)s[1]);
}
__device__ __forceinline__ float fold16(float a, float b) {
  auto s = __builtin_amdgcn_permlane16_swap(__builtin_bit_cast(unsigned, a), __builtin_bit_cast(unsigned, b), false, false);
  return __builtin_bit_cast(float, (unsigned)s[0]) + __builtin_bit_cast(float, (unsigned)s[1]);
}
__device__ __forceinline__ float row_sum(float v) {
  v += dpp<XOR1>(v); v += dpp<XOR2>(v); v += dpp<XOR7>(v); v += dpp<ROR8>(v); return v;
}
__device__ __forceinline__ float row_max(float v) {
  v = fmaxf(v, dpp<XOR1>(v)); v = fmaxf(v, dpp<XOR2>(v)); v = fmaxf(v, dpp<XOR7>(v)); v = fmaxf(v, dpp<ROR8>(v)); return v;
}
__device__ __forceinline__ float wave_sum(float v) { v = row_sum(v); v = fold16(v, v); return fold32(v, v); }
__device__ __forceinline__ float wave_max(float v) {
  v = row_max(v);
  auto s = __builtin_amdgcn_permlane16_swap(__builtin_bit_cast(unsigned, v), __builtin_bit_cast(unsigned, v), false, false);
  v = fmaxf(__builtin_bit_cast(float, (unsigned)s[0]), __builtin_bit_cast(float, (unsigned)s[1]));
  auto t = __builtin_amdgcn_permlane32_swap(__builtin_bit_cast(unsigned, v), __builtin_bit_cast(unsigned, v), false, false);
  return fmaxf(__builtin_bit_cast(float, (unsigned)t[0]), __builtin_bit_cast(float, (unsigned)t[1]));
}
struct Acc8 {
  f32x2 v[4];
  __device__ __forceinline__ void zero() { v[0] = v[1] = v[2] = v[3] = f32x2{0.f, 0.f}; }
  __device__ __forceinline__ float operator[](int i) const { return v[i >> 1][i & 1]; }
  __device__ __forceinline__ void pin() { asm volatile("" : "+v"(v[0]), "+v"(v[1]), "+v"(v[2]), "+v"(v[3])); }
};
__device__ __forceinline__ void fma8(Acc8& acc, float s, u32x4 v) {
  const f32x2 ss = {s, s};
#pragma unroll
  for (int k = 0; k < 4; ++k) acc.v[k] = __builtin_elementwise_fma(ss, f32x2{lo_f(v[k]), hi_f(v[k])}, acc.v[k]);
}
#define MEMBAR() asm volatile("" ::: "memory")
#define SB() __builtin_amdgcn_sched_barrier(0)
#define WAVE_SYNC() do { __builtin_amdgcn_fence(__ATOMIC_RELEASE, "wavefront"); __builtin_amdgcn_wave_barrier(); __builtin_amdgcn_fence(__ATOMIC_ACQUIRE, "wavefront"); } while (0)

#define STAMP(i) do {} while (0)
#define STAMP_OUT() do {} while (0)

#define ESTAMP(i) do {} while (0)
#define LSTAMP(i) STAMP(i)
template <int I> struct IC { static constexpr int value = I; };
template <int I, int N, class F> __device__ __forceinline__ void static_for(F&& f) {
  if constexpr (I < N) { f(IC<I>{}); static_for<I + 1, N>(f); }
}
__device__ __forceinline__ float transpose_reduce(float (&x)[16], int c) {
#define TR_STEP(HALF, CTRL) _Pragma("unroll") for (int i = 0; i < (HALF); ++i) { \
    const float lo_ = x[i], hi_ = x[i + (HALF)]; const float t1_ = lo_ + dpp<CTRL>(lo_), t2_ = hi_ + dpp<CTRL>(hi_); \
    x[i] = (c & (HALF)) ? t2_ : t1_; }
  TR_STEP(8, ROR8) TR_STEP(4, XOR7) TR_STEP(2, XOR2) TR_STEP(1, XOR1)
#undef TR_STEP
  return x[0];
}

constexpr int XP = 15, PRE = 24, D0 = PRE + 2, O0 = D0 + 18;
static_assert(O0 + 3 * 21 + 2 < 124 && !(RING & 1), "wo lands before its use");
constexpr int slot_at(int n, int first, int cnt) { return (n >= first && (n - first) % 3 == 0 && (n - first) / 3 < cnt) ? (n - first) / 3 : -1; }
constexpr double cost_before(int n) {
  double t = 0;
  for (int m = 0; m < n; ++m) {
    t += 1. + (m >= 2 && m < 7 ? .92 * CW : m == 7 || m == 8 ? 1.25 * CL : m == 9 ? 1.05 * CL : m == 10 ? CP : m == XP ? CX : 0.);
    t += (m == PRE + 1 ? SLACK0 : 0.) + (m == 64 + RING + 1 ? SLACKM : 0.);
  }
  return t;
}
constexpr double TAU = TAU_NS / 10.;
constexpr bool gated(int n) { return (n >= 2 && n <= 11) || (n > 11 && n <= XP ? (n & 1) != 0 : (n & 1) == 0); }
constexpr int gate_no(int n) { int g = 0; for (int m = 0; m < n; ++m) g += gated(m); return g; }
constexpr int target(int u) {
  const int drip = PRE + 2 * ((u + 2 * CATCH) / (2 * CATCH)), mid = u >= 64 ? 64 + RING + 2 : 0;
  int t = (u + 1 + RING) & ~1;
  t = t < drip ? drip : t; t = t < mid ? mid : t;
  return u < 0 ? PRE : (t > 128 ? 128 : t);
}
template <int DUE> __device__ __forceinline__ void gate(unsigned base) {
  asm volatile("1:\n\ts_memrealtime vcc\n\ts_waitcnt lgkmcnt(0)\n\ts_sub_u32 vcc_lo, vcc_lo, %0\n\ts_cmp_ge_i32 vcc_lo, %1\n\ts_cbranch_scc1 2f\n\ts_sleep 1\n\ts_branch 1b\n2:"
               :: "s"(base), "n"(DUE) : "vcc", "scc");
}

__global__ void __launch_bounds__(NTHR, 2)
fused_decode(const u16* x, const u16* __restrict__ kc, const u16* __restrict__ vc, const u16* __restrict__ wq,
             const u16* __restrict__ wo, const u16* __restrict__ wg, const u16* __restrict__ wu, const u16* __restrict__ wd,
             u16* __restrict__ out, unsigned char* ws) {
  __shared__ __attribute__((aligned(16))) float lds[L_END];
  const int tid = threadIdx.x, lane = tid & 63, w = __builtin_amdgcn_readfirstlane(tid >> 6);
  const int bid = blockIdx.x, j = bid >> 3;
  const int h = (bid & 7) * 4 + (j >> 3), b = (j & 7) >> 1, s = j & 1;
  const int pidx = 2 * h + s;
  const int row = lane >> 4, c = lane & 15;
  STAMP(0);
  const unsigned rid = (unsigned)(((b * NH + h) * 2 + s) * 8 + w);
  const rsrc_t rk = mk(kc + (size_t)rid * 32768, 65536);
  const unsigned rot = (rid ^ (rid >> 6)) & 63u;
  int voffs[4];
#pragma unroll
  for (int k = 0; k < 4; ++k) voffs[k] = lane * 16 + (int)((k ^ rot) & 3u) * 1024;
  const int rhi = (int)(rot >> 2) * 4096;
  u32x4 kv[128], wb[5], wdb[6], wob[22], rx4;
  unsigned tbase;
  {
    unsigned long long t0;
    asm volatile("s_memrealtime %0\n\ts_waitcnt lgkmcnt(0)" : "=s"(t0) : "s"(kc));
    tbase = (unsigned)t0 + (((unsigned)bid * (unsigned)(2 * TAU + .5)) >> 8);
  }
  gate<0>(tbase);
  kv[0] = ld16<AUX_K>(rk, voffs[0], rhi); kv[1] = ld16<AUX_K>(rk, voffs[1], rhi);
  SB();
  STAMP(6);
  unsigned xr[5];
  {
    const rsrc_t rxw = mk(x + (w & 3) * DM, w < 4 ? DM * 2 : 0);
#pragma unroll
    for (int i = 0; i < 5; ++i) xr[i] = __builtin_amdgcn_raw_buffer_load_b32(rxw, lane * 4 + i * 256, 0, AUX_W);
  }
  unsigned cw[3];
  {
    unsigned long long pc; unsigned len;
    asm volatile("s_getpc_b64 %0\n.Lfd_code0:\n\ts_mov_b32 %1, .Lfd_code1-.Lfd_code0" : "=s"(pc), "=s"(len));
    const rsrc_t rc = mk((const void*)pc, (j & 15) == 0 ? len : 0u);
#pragma unroll
    for (int i = 0; i < 3; ++i) cw[i] = __builtin_amdgcn_raw_buffer_load_b32(rc, w == 5 ? lane * 128 : OOB, i * 8192, 0);
  }
  const rsrc_t rws = mk(ws, WS_END);
  const unsigned epoch = __builtin_amdgcn_raw_buffer_load_b32(rws, WS_EPOCH, 0, AUX_SC1);
  MEMBAR();
  SB();
  const rsrc_t rv = mk(vc + (size_t)rid * 32768, 65536);
  const int fb = (j >> 3) * 8 + (bid & 7);
  const bool ffn = fb < FF / 64;
  const int pc = ffn ? fb : 0;
  const rsrc_t ro = mk(wo + (size_t)h * DH * DM, DH * DM * 2);
  const rsrc_t rd = mk(wd + (size_t)(2 * pc + s) * 32 * DM, ffn ? 32 * DM * 2 : 0);
  const int rr = tid / 80;
  const int pcs = tid < 480 ? tid * 16 : OOB;
  const int pco = tid < 480 ? tid * 16 + rr * 1280 : OOB;
  const int xoff = WS_X + ((h * NB + b) * 8 + w) * 1024;
  const bool xuse = ffn || w < 4;
  const int krow = 160 * b + 40 * (w & 3) + (lane >> 3);
  const int pitch = w < 4 ? NH * DH * 2 : FF * 2, col = w < 4 ? h * DH + s * 64 : pc * 64;
  const rsrc_t rw = mk((w < 4 ? wq : s ? wu : wg) + col, xuse ? (unsigned)(DM * pitch - col * 2) : 0u);
  const int vw = krow * pitch + (lane & 7) * 16;
  unsigned lw[3];
  auto issue = [&](auto I) {
    constexpr int n = decltype(I)::value;
    if constexpr (gated(n)) {
      constexpr int due = (int)(TAU * cost_before(n));
      gate<due>(tbase);
    }
    if constexpr (n < 64) kv[n] = ld16<AUX_K>(rk, voffs[n & 3], rhi ^ ((n >> 2) * 4096));
    else kv[n] = ld16<AUX_V>(rv, voffs[n & 3], rhi ^ (((n & 63) >> 2) * 4096));
    SB();
    constexpr int id = slot_at(n, D0, 6), io = slot_at(n, O0, 22);
    if constexpr (n >= 2 && n < 7) wb[n - 2] = ld16<AUX_NT>(rw, vw, (n - 2) * 8 * pitch);
    if constexpr (n == 7 || n == 8) lw[n - 7] = __builtin_amdgcn_raw_buffer_load_b32(ro, lane < 10 ? lane * 128 : OOB, (((j & 7) * 8 + w) * 2 + n - 7) * 1280, AUX_W);
    if constexpr (n == 9) lw[2] = __builtin_amdgcn_raw_buffer_load_b32(rd, lane < 10 ? lane * 128 : OOB, (b * 8 + w) * 1280, AUX_W);
    if constexpr (n == XP) { MEMBAR(); rx4 = ld16<AUX_SC1>(rws, xuse ? lane * 16 : OOB, xoff); }
    if constexpr (id >= 0) wdb[id] = ld16<AUX_W>(rd, pcs, id * 7680);
    if constexpr (io >= 0) wob[io] = ld16<AUX_W>(ro, pco + (io & 1) * 1280, (io >> 1) * 15360);
    SB();
  };
  static_for<2, 4>(issue);
  if (w < 4) {
    float xv[10], sum = 0.f;
#pragma unroll
    for (int i = 0; i < 5; ++i) { xv[2 * i] = lo_f(xr[i]); xv[2 * i + 1] = hi_f(xr[i]); sum += xv[2 * i] + xv[2 * i + 1]; }
    const float mean = wave_sum(sum) * (1.f / DM);
    float sq = 0.f;
#pragma unroll
    for (int i = 0; i < 10; ++i) { const float d = xv[i] - mean; sq = fmaf(d, d, sq); }
    const float var = bfr(wave_sum(sq) * (1.f / DM));
    const float mb = bfr(mean), rs = 1.f / bfr(sqrtf(bfr(var + 1e-5f)));
#pragma unroll
    for (int i = 0; i < 10; ++i) lds[L_XN + (2 * lane + 128 * (i >> 1) + (i & 1)) * 4 + w] = bfr(bfr(xv[i] - mb) * rs);
  }
  SB();
  static_for<4, 6>(issue);
  __syncthreads();
  asm volatile("" :: "v"(cw[0]), "v"(cw[1]), "v"(cw[2]));
  const unsigned tag0 = epoch + 1u, tag1 = (epoch + 1u) * 0x9E3779B1u + 0x7F4A7C15u;
  {
    Acc8 a[4];
#pragma unroll
    for (int bb = 0; bb < 4; ++bb) a[bb].zero();
    static_for<0, 5>([&](auto T) {
      constexpr int t = decltype(T)::value;
      const f32x4 xn4 = *(const f32x4*)(lds + L_XN + (krow + 8 * t) * 4);
#pragma unroll
      for (int bb = 0; bb < 4; ++bb) { fma8(a[bb], xn4[bb], wb[t]); a[bb].pin(); }
      SB();
      if constexpr (t >= 1 && t < 4) issue(IC<5 + t>{});
    });
    float v[8];
#pragma unroll
    for (int k = 0; k < 8; ++k) { v[k] = fold16(fold32(a[0][k], a[2][k]), fold32(a[1][k], a[3][k])); v[k] += dpp<ROR8>(v[k]); }
    const bool up = lane & 8;
    *(f32x4*)(lds + L_PP + w * 256 + row * 64 + (lane & 7) * 8 + (up ? 4 : 0)) = f32x4{up ? v[4] : v[0], up ? v[5] : v[1], up ? v[6] : v[2], up ? v[7] : v[3]};
  }
  SB();
  issue(IC<9>{});
  __syncthreads();
  const int slab = WS_SLAB + (b * 64 + pidx) * (DM * 4);
  if (w < 4) {
    f32x2 v = {0.f, 0.f};
#pragma unroll
    for (int i = 0; i < 4; ++i) v += *(const f32x2*)(lds + L_PP + ((tid >> 7) * 4 + i) * 256 + 2 * (tid & 127));
    const u32x4 r = {__float_as_uint(v[0]), tag0, __float_as_uint(v[1]), tag1};
    const int o = (((tid >> 5) & 3) * 8 + (tid >> 7) * 4 + b) * 1024 + s * 512 + (tid & 31) * 16;
    __builtin_amdgcn_raw_buffer_store_b128(r, rws, (ffn || w < 2) ? o : OOB, WS_X + h * NB * 8192, AUX_SC1);
  } else if (w == 7) {
    const u32x4 sv = {SENT, SENT, SENT, SENT};
    __builtin_amdgcn_raw_buffer_store_b128(sv, rws, lane * 16, slab, AUX_SC1);
    __builtin_amdgcn_raw_buffer_store_b128(sv, rws, lane * 16 + 1024, slab, AUX_SC1);
    __builtin_amdgcn_raw_buffer_store_b128(sv, rws, lane < 32 ? lane * 16 + 2048 : OOB, slab, AUX_SC1);
  }
  SB();
  STAMP(7);
  static_for<10, PRE - 2>(issue);
  asm volatile("" :: "v"(lw[0]), "v"(lw[1]), "v"(lw[2]));
  if (__builtin_expect(xuse && __builtin_amdgcn_ballot_w64(((rx4[1] ^ tag0) | (rx4[3] ^ tag1)) != 0u) != 0ull, 0)) {
    unsigned spin = 0;
    do {
      MEMBAR();
      rx4 = ld16<AUX_SC1>(rws, lane * 16, xoff);
      __builtin_amdgcn_s_waitcnt(0x0F70);
    } while (__builtin_amdgcn_ballot_w64(((rx4[1] ^ tag0) | (rx4[3] ^ tag1)) != 0u) != 0ull && ++spin < SPIN_CAP);
  }
  *(f32x2*)(lds + L_QP + w * 128 + 2 * lane) = f32x2{__uint_as_float(rx4[0]), __uint_as_float(rx4[2])};
  SB();
  static_for<PRE - 2, PRE>(issue);
  __syncthreads();
  u32x4 q;
  {
    f32x4 s0 = {0.f, 0.f, 0.f, 0.f}, s1 = {0.f, 0.f, 0.f, 0.f};
    float gu = 0.f;
#pragma unroll
    for (int kq = 0; kq < 4; ++kq) {
      s0 += *(const f32x4*)(lds + L_QP + kq * 128 + 8 * c); s1 += *(const f32x4*)(lds + L_QP + kq * 128 + 8 * c + 4);
      gu += lds[L_QP + (4 + kq) * 128 + (lane >> 5) * 64 + 32 * s + (lane & 31)];
    }
    q[0] = pk_bf16(s0[0], s0[1]); q[1] = pk_bf16(s0[2], s0[3]); q[2] = pk_bf16(s1[0], s1[1]); q[3] = pk_bf16(s1[2], s1[3]);
    gu = bfr(gu);
    auto sw = __builtin_amdgcn_permlane32_swap(__float_as_uint(gu), __float_as_uint(gu), false, false);
    const float hv = bfr(__uint_as_float(sw[0]) * __uint_as_float(sw[1]));
    if (lane < 48) lds[L_HH + w * 48 + lane] = lane < 32 ? hv : 0.f;
    WAVE_SYNC();
  }
  STAMP(1);

  Acc8 y; y.zero();
  auto side = [&](auto U) {
    constexpr int u = decltype(U)::value;
    if constexpr (u >= 20 && u < 84 && (u & 7) == 4) {
      if (w == 7 && (j & 7) == (u - 20) / 8) {
        asm volatile("s_waitcnt vmcnt(0)" ::: "memory");
        __builtin_amdgcn_raw_buffer_store_b128(u32x4{0u, tag0, 0u, tag1}, rws, lane == 0 ? 0 : OOB, WS_FLAG + (b * 64 + pidx) * 16, AUX_SC1);
      }
      SB();
    }
    if constexpr (slot_at(u - 1, D0, 6) >= 0) {
      constexpr int i = slot_at(u - 1, D0, 6);
      fma8(y, lds[L_HH + w * 48 + rr + 6 * i], wdb[i]);
      y.pin();
      SB();
    }
    if constexpr (u >= O0 + 4 && (u - O0 - 4) % 6 == 0 && (u - O0 - 4) / 6 < 11) {
      constexpr int i = (u - O0 - 4) / 6;
#pragma unroll
      for (int k = 0; k < 4; ++k) {
        const unsigned a_ = wob[2 * i][k], b_ = wob[2 * i + 1][k];
        wob[2 * i][k] = __builtin_amdgcn_perm(b_, a_, 0x05040100u);
        wob[2 * i + 1][k] = __builtin_amdgcn_perm(b_, a_, 0x07060302u);
      }
      asm volatile("" : "+v"(wob[2 * i]), "+v"(wob[2 * i + 1]));
      SB();
    }
  };

  constexpr float SCALE = 0.08838834764831845f, LOG2E = 1.4426950408889634f;
  float sc[4];
  {
    float xx[16];
    static_for<0, 64>([&](auto I) {
      constexpr int st = decltype(I)::value;
      float d = dot2(kv[st][0], q[0], 0.f);
      d = dot2(kv[st][1], q[1], d); d = dot2(kv[st][2], q[2], d); d = dot2(kv[st][3], q[3], d);
      asm volatile("" : "+v"(d));
      xx[st & 15] = d;
      SB();
      static_for<target(st - 1), target(st)>(issue);
      side(I);
      if constexpr ((st & 15) == 15) { sc[st >> 4] = bfr(bfr(transpose_reduce(xx, c)) * SCALE); SB(); }
    });
  }
  LSTAMP(2);
  float mw = wave_max(fmaxf(fmaxf(sc[0], sc[1]), fmaxf(sc[2], sc[3])));
  asm volatile("" : "+v"(mw));
  SB();
  static_for<target(63), target(64)>(issue);
  float p[4];
#pragma unroll
  for (int g = 0; g < 4; ++g) p[g] = __builtin_amdgcn_exp2f((sc[g] - mw) * LOG2E);
  const float lsum = wave_sum((p[0] + p[1]) + (p[2] + p[3]));
  const int rec = WS_REC + (b * NH + h) * 256;
  {
    const u32x4 r = {__float_as_uint(mw), tag0, __float_as_uint(lsum), tag1};
    __builtin_amdgcn_raw_buffer_store_b128(r, rws, lane == 0 ? (s * 8 + w) * 16 : OOB, rec, AUX_SC1);
  }
  SB();

  Acc8 acc; acc.zero();
  const int cg = pidx - 24;
  const bool red = w == 7 && cg >= 0;
  const int prec = red ? WS_FLAG + (b * 64 + lane) * 16 : OOB;
  u32x4 r1, r2;
  unsigned xo = 0;
  float fw = 0.f;
  bool ok2 = false;
  static_for<0, 64>([&](auto I) {
    constexpr int st = decltype(I)::value;
    fma8(acc, dpp<BCAST + (st & 15)>(p[st >> 4]), kv[64 + st]);
    acc.pin();
    SB();
    if constexpr (st > 0) static_for<target(63 + st), target(64 + st)>(issue);
    side(IC<64 + st>{});
    if constexpr (st == RLD) { MEMBAR(); r1 = ld16<AUX_SC1>(rws, c * 16, rec); SB(); }
    if constexpr (st == RLD + 2) { MEMBAR(); r2 = ld16<AUX_SC1>(rws, prec, 0); SB(); }
    if constexpr (st == RLD + 4) { xo = __builtin_amdgcn_raw_buffer_load_b16(mk(x + b * DM, red ? DM * 2 : 0), (cg * 16 + 4 * (lane & 3) + row) * 2, 0, AUX_W); SB(); }
    if constexpr (st == RLD + RING + 4) {
      if (__builtin_expect(__builtin_amdgcn_ballot_w64(((r1[1] ^ tag0) | (r1[3] ^ tag1)) != 0u) != 0ull, 0)) {
        unsigned spin = 0;
        do {
          __builtin_amdgcn_s_sleep(8); MEMBAR();
          r1 = ld16<AUX_SC1>(rws, c * 16, rec);
          __builtin_amdgcn_s_waitcnt(0x0F70);
        } while (__builtin_amdgcn_ballot_w64(((r1[1] ^ tag0) | (r1[3] ^ tag1)) != 0u) != 0ull && ++spin < SPIN_CAP);
      }
      ok2 = __builtin_amdgcn_ballot_w64(((r2[1] ^ tag0) | (r2[3] ^ tag1)) != 0u) == 0ull;
      asm volatile("" :: "v"(r2));
      const float mi = __uint_as_float(r1[0]), li = __uint_as_float(r1[2]);
      const float M = row_max(mi);
      const float L = row_sum(li * __builtin_amdgcn_exp2f((mi - M) * LOG2E));
      fw = __builtin_amdgcn_exp2f((mw - M) * LOG2E) / L;
      asm volatile("" : "+v"(fw));
      SB();
    }
  });
  LSTAMP(3);
  {
    const float e0 = fold32(acc[0], acc[4]), e1 = fold32(acc[1], acc[5]), e2 = fold32(acc[2], acc[6]), e3 = fold32(acc[3], acc[7]);
    const f32x2 o = {fold16(e0, e2) * fw, fold16(e1, e3) * fw};
    *(f32x2*)(lds + L_OP + w * 128 + 8 * c + 2 * row) = o;
  }
  __syncthreads();
  {
    f32x2 o = {0.f, 0.f};
#pragma unroll
    for (int ww = 0; ww < 8; ++ww) o += *(const f32x2*)(lds + L_OP + ww * 128 + 2 * lane);
    unsigned* ot = (unsigned*)lds + L_OT + w * 72;
    ot[(lane % 6) * 12 + lane / 6] = pk_bf16(o[0], o[1]);
    if (lane < 2) ot[(4 + lane) * 12 + 10] = 0u;
    WAVE_SYNC();
    const unsigned* orow = ot + (rr < 6 ? rr : 0) * 12;
    float yy[8];
#pragma unroll
    for (int k = 0; k < 8; ++k) yy[k] = y[k];
#pragma unroll
    for (int i4 = 0; i4 < 3; ++i4) {
      const u32x4 ov = *(const u32x4*)(orow + 4 * i4);
#pragma unroll
      for (int e = 0; e < 4; ++e) {
        const int i = 4 * i4 + e;
        if (i < 11) {
#pragma unroll
          for (int k = 0; k < 4; ++k) { yy[2 * k] = dot2(wob[2 * i][k], ov[e], yy[2 * k]); yy[2 * k + 1] = dot2(wob[2 * i + 1][k], ov[e], yy[2 * k + 1]); }
        }
      }
    }
    if (tid < 480) {
      *(f32x4*)(lds + L_YP + tid * 8) = f32x4{yy[0], yy[1], yy[2], yy[3]};
      *(f32x4*)(lds + L_YP + tid * 8 + 4) = f32x4{yy[4], yy[5], yy[6], yy[7]};
    }
  }
  __syncthreads();
  if (tid < 160) {
    f32x4 v = {0.f, 0.f, 0.f, 0.f};
#pragma unroll
    for (int i = 0; i < 6; ++i) v += *(const f32x4*)(lds + L_YP + i * 640 + tid * 4);
    u32x4 u = __builtin_bit_cast(u32x4, v);
#pragma unroll
    for (int k = 0; k < 4; ++k) u[k] = u[k] == SENT ? 0x7FC00000u : u[k];
    __builtin_amdgcn_raw_buffer_store_b128(u, rws, tid * 16, slab, AUX_SC1);
  }
  LSTAMP(4);

  if (red) {
    for (unsigned spin = 0; !ok2 && spin < SPIN_CAP; ++spin) {
      MEMBAR();
      r2 = ld16<AUX_SC1>(rws, prec, 0);
      ok2 = __builtin_amdgcn_ballot_w64(((r2[1] ^ tag0) | (r2[3] ^ tag1)) != 0u) == 0ull;
    }
    const int so = WS_SLAB + b * 64 * DM * 4 + cg * 64, vo = (lane >> 2) * (DM * 4) + (lane & 3) * 16;
    u32x4 pv[4], pt[NPOLL][4];
    int po[4];
    const u32x4 srd = {(unsigned)(uintptr_t)ws, (unsigned)((uintptr_t)ws >> 32) & 0xffffu, (unsigned)WS_END, (unsigned)RSRC_W3};
    const int so0 = so, so1 = so + 16 * DM * 4, so2 = so + 32 * DM * 4, so3 = so + 48 * DM * 4;
#define POLL(d) asm volatile("buffer_load_dwordx4 %0, %4, %8, %9 offen sc1\n\tbuffer_load_dwordx4 %1, %5, %8, %10 offen sc1\n\t" \
                             "buffer_load_dwordx4 %2, %6, %8, %11 offen sc1\n\tbuffer_load_dwordx4 %3, %7, %8, %12 offen sc1" \
                             : "+v"(pt[d][0]), "+v"(pt[d][1]), "+v"(pt[d][2]), "+v"(pt[d][3]) \
                             : "v"(po[0]), "v"(po[1]), "v"(po[2]), "v"(po[3]), "s"(srd), "s"(so0), "s"(so1), "s"(so2), "s"(so3) : "memory")
#pragma unroll
    for (int i = 0; i < 4; ++i) {
      pv[i] = u32x4{0u, 0u, 0u, 0u}; po[i] = vo;
#pragma unroll
      for (int d = 0; d < NPOLL; ++d) pt[d][i] = u32x4{0u, 0u, 0u, 0u};
    }
    asm volatile("s_waitcnt vmcnt(0)" ::: "memory");
#pragma unroll
    for (int d = 0; d < NPOLL; ++d) { POLL(d); if (d + 1 < NPOLL) __builtin_amdgcn_s_sleep(4); }
    for (unsigned spin = 0; spin < SPIN_CAP; ++spin) {
#pragma unroll
      for (int d = 0; d < NPOLL; ++d) {
        asm volatile("s_waitcnt vmcnt(%4)" : "+v"(pt[d][0]), "+v"(pt[d][1]), "+v"(pt[d][2]), "+v"(pt[d][3]) : "n"(4 * (NPOLL - 1)) : "memory");
#pragma unroll
        for (int i = 0; i < 4; ++i) {
          const u32x4 t = pt[d][i];
          const unsigned mx = max(max(t[0], t[1]), max(t[2], t[3])) | (po[i] == vo ? 0u : SENT);
          const bool got = mx != SENT;
          pv[i] = got ? t : pv[i];
          po[i] = got ? OOB : po[i];
        }
        if (__builtin_amdgcn_ballot_w64((po[0] & po[1] & po[2] & po[3] & OOB) == 0) == 0ull) goto settled;
        POLL(d);
      }
    }
  settled:;
    const f32x4 f = (__builtin_bit_cast(f32x4, pv[0]) + __builtin_bit_cast(f32x4, pv[1])) + (__builtin_bit_cast(f32x4, pv[2]) + __builtin_bit_cast(f32x4, pv[3]));
    const float e0 = fold32(f[0], f[2]), e1 = fold32(f[1], f[3]);
    float v = fold16(e0, e1);
    v += dpp<ROR8>(v); v += dpp<ROR4>(v);
    __builtin_amdgcn_raw_buffer_store_b16(__builtin_bit_cast(u16, (__bf16)(lo_f(xo) + v)), mk(out, NB * DM * 2), c < 4 ? (b * DM + cg * 16 + 4 * c + row) * 2 : OOB, 0, 0);
    if (lane == 0 && cg == 0 && b == 0) __builtin_amdgcn_raw_buffer_store_b32(tag0, rws, WS_EPOCH, 0, AUX_SC1);
    STAMP(5);
    SB();
#pragma unroll
    for (int d = 0; d < NPOLL; ++d) asm volatile("s_waitcnt vmcnt(0)" :: "v"(pt[d][0]), "v"(pt[d][1]), "v"(pt[d][2]), "v"(pt[d][3]) : "memory");
  }
  STAMP_OUT();
  asm volatile(".Lfd_code1:");
}

}

extern "C" void kernel_launch(void* const* d_in, const int* in_sizes, int n_in, void* d_out, int out_size, void* d_ws, size_t ws_size,
                              hipStream_t stream) {
  (void)in_sizes; (void)n_in; (void)out_size; (void)ws_size;
  fused_decode<<<NWG, NTHR, 0, stream>>>((const u16*)d_in[0], (const u16*)d_in[1], (const u16*)d_in[2], (const u16*)d_in[3],
                                         (const u16*)d_in[4], (const u16*)d_in[6], (const u16*)d_in[7], (const u16*)d_in[8],
                                         (u16*)d_out, (unsigned char*)d_ws);
}
```

```cpp
#include <hip/hip_runtime.h>
#include <hip/hip_bf16.h>
#include <stdint.h>

namespace {

constexpr int NB = 4, DM = 640, NH = 32, SK = 4096, DH = 128, FF = 1728;
constexpr int NWG = 256, NTHR = 512;
#define TAU_NS 271.
#define CW 1.15
#define CL 1.15
#define CP .6
#define CX 1.15
#define SLACK0 1.
#define SLACKM 0.25
#define RING 8
#define RLD 40
#define CATCH 4
#define PAT 3
#define PD0 145
#define PD1 6
#define SW 0
#define SYNC 0
#define T0ALIGN ""
#define RB0 0
#define KWARM 1
#define PSK 0
#define AUXW 0

typedef unsigned short u16;
typedef unsigned u32x4 __attribute__((ext_vector_type(4)));
typedef unsigned u32x2 __attribute__((ext_vector_type(2)));
typedef float f32x4 __attribute__((ext_vector_type(4)));
typedef float f32x2 __attribute__((ext_vector_type(2)));
typedef __bf16 bf16x2 __attribute__((ext_vector_type(2)));
typedef short s16x4 __attribute__((ext_vector_type(4)));
typedef __bf16 bf16x8 __attribute__((ext_vector_type(8)));
typedef __amdgpu_buffer_rsrc_t rsrc_t;

constexpr int AUX_K = 2, AUX_V = 2, AUX_W = AUXW, AUX_NT = 2, AUX_SC1 = 16, RSRC_W3 = 0x00020000;

constexpr int WS_EPOCH = 0, WS_T0 = 2048, WS_REC = 4096, WS_SLAB = WS_REC + NB * NH * 16 * 16, WS_X = WS_SLAB + NB * 64 * DM * 8, WS_END = WS_X + NH * NB * 8192;
constexpr int WS_STAMP = 4 << 20;
constexpr unsigned SPIN_CAP = 1u << 18;
constexpr int OOB = 1 << 28;

constexpr int L_XN = 0;
constexpr int L_PP = L_XN + 640 * 4;
constexpr int L_QP = L_PP + 8 * 256;
constexpr int L_HH = L_QP + 8 * 128;
constexpr int L_OP = L_HH + 8 * 16;
constexpr int L_ST = L_OP + 8 * 72;
constexpr int STP = 328;
constexpr int L_END = L_ST + 2 * 32 * STP;

__device__ __forceinline__ rsrc_t mk(const void* p, unsigned bytes) {
  return __builtin_amdgcn_make_buffer_rsrc(const_cast<void*>(p), 0, (int)bytes, RSRC_W3);
}
template <int AUX> __device__ __forceinline__ u32x4 ld16(rsrc_t r, int voff, int soff) {
  return __builtin_amdgcn_raw_buffer_load_b128(r, voff, soff, AUX);
}
__device__ __forceinline__ float lo_f(unsigned u) { return __builtin_bit_cast(float, u << 16); }
__device__ __forceinline__ float hi_f(unsigned u) { return __builtin_bit_cast(float, u & 0xffff0000u); }
__device__ __forceinline__ float bfr(float f) { return (float)(__bf16)f; }
__device__ __forceinline__ unsigned pk_bf16(float lo, float hi) {
  bf16x2 v = {(__bf16)lo, (__bf16)hi};
  return __builtin_bit_cast(unsigned, v);
}
__device__ __forceinline__ float dot2(unsigned a, unsigned b, float acc) {
  return __builtin_amdgcn_fdot2_f32_bf16(__builtin_bit_cast(bf16x2, a), __builtin_bit_cast(bf16x2, b), acc, false);
}
template <int CTRL> __device__ __forceinline__ float dpp(float x) {
  return __builtin_bit_cast(float, __builtin_amdgcn_mov_dpp(__builtin_bit_cast(int, x), CTRL, 0xf, 0xf, true));
}
constexpr int XOR1 = 0xB1, XOR2 = 0x4E, XOR7 = 0x141, ROR8 = 0x128, ROR4 = 0x124, BCAST = 0x150;
__device__ __forceinline__ float fold32(float a, float b) {
  auto s = __builtin_amdgcn_permlane32_swap(__builtin_bit_cast(unsigned, a), __builtin_bit_cast(unsigned, b), false, false);
  return __builtin_bit_cast(float, (unsigned)s[0]) + __builtin_bit_cast(float, (unsigned)s[1]);
}
__device__ __forceinline__ float fold16(float a, float b) {
  auto s = __builtin_amdgcn_permlane16_swap(__builtin_bit_cast(unsigned, a), __builtin_bit_cast(unsigned, b), false, false);
  return __builtin_bit_cast(float, (unsigned)s[0]) + __builtin_bit_cast(float, (unsigned)s[1]);
}
__device__ __forceinline__ float row_sum(float v) {
  v += dpp<XOR1>(v); v += dpp<XOR2>(v); v += dpp<XOR7>(v); v += dpp<ROR8>(v); return v;
}
__device__ __forceinline__ float row_max(float v) {
  v = fmaxf(v, dpp<XOR1>(v)); v = fmaxf(v, dpp<XOR2>(v)); v = fmaxf(v, dpp<XOR7>(v)); v = fmaxf(v, dpp<ROR8>(v)); return v;
}
__device__ __forceinline__ float wave_sum(float v) { v = row_sum(v); v = fold16(v, v); return fold32(v, v); }
__device__ __forceinline__ float wave_max(float v) {
  v = row_max(v);
  auto s = __builtin_amdgcn_permlane16_swap(__builtin_bit_cast(unsigned, v), __builtin_bit_cast(unsigned, v), false, false);
  v = fmaxf(__builtin_bit_cast(float, (unsigned)s[0]), __builtin_bit_cast(float, (unsigned)s[1]));
  auto t = __builtin_amdgcn_permlane32_swap(__builtin_bit_cast(unsigned, v), __builtin_bit_cast(unsigned, v), false, false);
  return fmaxf(__builtin_bit_cast(float, (unsigned)t[0]), __builtin_bit_cast(float, (unsigned)t[1]));
}
struct Acc8 {
  f32x2 v[4];
  __device__ __forceinline__ void zero() { v[0] = v[1] = v[2] = v[3] = f32x2{0.f, 0.f}; }
  __device__ __forceinline__ float operator[](int i) const { return v[i >> 1][i & 1]; }
  __device__ __forceinline__ void pin() { asm volatile("" : "+v"(v[0]), "+v"(v[1]), "+v"(v[2]), "+v"(v[3])); }
};
__device__ __forceinline__ void fma8(Acc8& acc, float s, u32x4 v) {
  const f32x2 ss = {s, s};
#pragma unroll
  for (int k = 0; k < 4; ++k) acc.v[k] = __builtin_elementwise_fma(ss, f32x2{lo_f(v[k]), hi_f(v[k])}, acc.v[k]);
}
#define MEMBAR() asm volatile("" ::: "memory")
#define SB() __builtin_amdgcn_sched_barrier(0)
#define WAVE_SYNC() do { __builtin_amdgcn_fence(__ATOMIC_RELEASE, "wavefront"); __builtin_amdgcn_wave_barrier(); __builtin_amdgcn_fence(__ATOMIC_ACQUIRE, "wavefront"); } while (0)

#define STAMP(i) do {} while (0)
#define STAMP_OUT() do {} while (0)

#define ESTAMP(i) do {} while (0)
#define LSTAMP(i) STAMP(i)
template <int I> struct IC { static constexpr int value = I; };
template <int I, int N, class F> __device__ __forceinline__ void static_for(F&& f) {
  if constexpr (I < N) { f(IC<I>{}); static_for<I + 1, N>(f); }
}
__device__ __forceinline__ float transpose_reduce(float (&x)[16], int c) {
#define TR_STEP(HALF, CTRL) _Pragma("unroll") for (int i = 0; i < (HALF); ++i) { \
    const float lo_ = x[i], hi_ = x[i + (HALF)]; const float t1_ = lo_ + dpp<CTRL>(lo_), t2_ = hi_ + dpp<CTRL>(hi_); \
    x[i] = (c & (HALF)) ? t2_ : t1_; }
  TR_STEP(8, ROR8) TR_STEP(4, XOR7) TR_STEP(2, XOR2) TR_STEP(1, XOR1)
#undef TR_STEP
  return x[0];
}

constexpr int XP = 15, PRE = 24, D0 = PRE + 2;
static_assert(D0 + 3 * 24 + 4 < 124 && !(RING & 1), "wo lands before its use");
constexpr int slot_at(int n, int first, int cnt) { return (n >= first && (n - first) % 3 == 0 && (n - first) / 3 < cnt) ? (n - first) / 3 : -1; }
constexpr double TAU = TAU_NS / 10.;
constexpr bool gated(int n) { return (n >= 2 && n <= 11) || (n > 11 && n <= XP ? (n & 1) != 0 : (n & 1) == 0); }
constexpr int gate_no(int n) { int g = 0; for (int m = 0; m < n; ++m) g += gated(m); return g; }
constexpr int EXTRA[18] = {0, 2, 2, 2, 2, 2, 10, 10, 2, -11, 4, 0, -9, 0, 0, 0, 0, 4};
constexpr int due_at(int n) { double t = 0; for (int g = 0; g < gate_no(n); ++g) t += 2 * TAU + (g < 18 ? EXTRA[g] : g == 41 ? 4 : 0); return (int)t; }
constexpr int target(int u) {
  const int drip = PRE + 2 * ((u + 2 * CATCH) / (2 * CATCH)), mid = u >= 64 ? 64 + RING + 2 : 0;
  int t = (u + 1 + RING) & ~1;
  t = t < drip ? drip : t; t = t < mid ? mid : t;
  return u < 0 ? PRE : (t > 128 ? 128 : t);
}
constexpr int RCOL[12] = {33, 62, 8, 25, 32, 9, 36, 28, 40, 48, 41, 0}, OCOL[6] = {33, 21, 1, 16, 57, 18};
constexpr int ord(int n) { int r = 0; for (int k = 0; k < 6; ++k) if ((n >> k) & 1) r ^= OCOL[k]; return r; }
constexpr unsigned long long rtab(int k0) { unsigned long long t = 0; for (int i = 0; i < 8; ++i) { int r = 0; for (int k = 0; k < 3; ++k) if ((i >> k) & 1) r ^= RCOL[k0 + k]; t |= (unsigned long long)r << (8 * i); } return t; }
template <int DUE> __device__ __forceinline__ void gate(unsigned base) {
  asm volatile("1:\n\ts_memrealtime vcc\n\ts_waitcnt lgkmcnt(0)\n\ts_sub_u32 vcc_lo, vcc_lo, %0\n\ts_cmp_ge_i32 vcc_lo, %1\n\ts_cbranch_scc1 2f\n\ts_sleep 1\n\ts_branch 1b\n2:"
               :: "s"(base), "n"(DUE) : "vcc", "scc");
}

__global__ void __launch_bounds__(NTHR, 2)
fused_decode(const u16* x, const u16* __restrict__ kc, const u16* __restrict__ vc, const u16* __restrict__ wq,
             const u16* __restrict__ wo, const u16* __restrict__ wg, const u16* __restrict__ wu, const u16* __restrict__ wd,
             u16* __restrict__ out, unsigned char* ws) {
  __shared__ __attribute__((aligned(16))) float lds[L_END];
  SB();
  const int tid = threadIdx.x, lane = tid & 63, w = __builtin_amdgcn_readfirstlane(tid >> 6);
  const int bid = blockIdx.x, j = bid >> 3;
  const int h = (bid & 7) * 4 + (j >> 3), b = (j & 7) >> 1, s = j & 1;
  const int pidx = 2 * h + s;
  const int row = lane >> 4, c = lane & 15;
  const unsigned rid = (unsigned)(((b * NH + h) * 2 + s) * 8 + w);
  const unsigned rot = (unsigned)((rtab(0) >> (8 * (rid & 7))) ^ (rtab(3) >> (8 * ((rid >> 3) & 7))) ^ (rtab(6) >> (8 * ((rid >> 6) & 7))) ^ (rtab(9) >> (8 * (rid >> 9)))) & 63u;
  int voffs[4];
#pragma unroll
  for (int k = 0; k < 4; ++k) voffs[k] = lane * 16 + (int)((k ^ rot) & 3u) * 1024;
  const int rhi = (int)(rot >> 2) * 4096;
  u32x4 kv[128], wb[5], wst[25], rx4;
  unsigned tbase;
  {
    const unsigned pord = ((unsigned)bid & 0x3Eu) | (((unsigned)bid & 1u) << 6) | (((unsigned)bid & 64u) << 1) | ((unsigned)bid >> 7);
    const unsigned phi = (pord * (unsigned)(2 * TAU + .5)) >> 8;
    const int r1_ = rhi ^ ((ord(1) >> 2) * 4096);
    asm volatile("" :: "s"(phi), "s"(rhi), "s"(r1_), "v"(voffs[0]), "v"(voffs[ord(1) & 3]));
    SB();
    asm volatile(T0ALIGN "s_memrealtime vcc\n\ts_waitcnt lgkmcnt(0)\n\ts_add_u32 %0, vcc_lo, %1" : "=s"(tbase) : "s"(phi), "s"(kc), "s"(wd), "s"(ws), "s"(rhi), "s"(r1_), "v"(voffs[0]), "v"(voffs[ord(1) & 3]) : "vcc", "scc");
  }
  const rsrc_t rk = mk(kc + (size_t)rid * 32768, 65536);
  gate<0>(tbase);
  kv[0] = ld16<AUX_K>(rk, voffs[0], rhi); kv[1] = ld16<AUX_K>(rk, voffs[ord(1) & 3], rhi ^ ((ord(1) >> 2) * 4096));
  asm volatile("" : "+s"(x), "+s"(vc), "+s"(wq), "+s"(wo), "+s"(wg), "+s"(wu), "+s"(wd), "+s"(ws));
  SB();
  STAMP(6);
  unsigned xr[5];
  {
    const rsrc_t rxw = mk(x + (w & 3) * DM, w < 4 ? DM * 2 : 0);
#pragma unroll
    for (int i = 0; i < 5; ++i) xr[i] = __builtin_amdgcn_raw_buffer_load_b32(rxw, lane * 4 + i * 256, 0, AUX_W);
  }
  unsigned cw[3];
  {
    unsigned long long pc; unsigned len;
    asm volatile("s_getpc_b64 %0\n.Lfd_code0:\n\ts_mov_b32 %1, .Lfd_code1-.Lfd_code0" : "=s"(pc), "=s"(len));
    const rsrc_t rc = mk((const void*)pc, (j & 15) == 0 ? len : 0u);
#pragma unroll
    for (int i = 0; i < 3; ++i) cw[i] = __builtin_amdgcn_raw_buffer_load_b32(rc, w == 5 ? lane * 128 : OOB, i * 8192, 0);
  }
  const rsrc_t rws = mk(ws, WS_END);
  const unsigned epoch = __builtin_amdgcn_raw_buffer_load_b32(rws, WS_EPOCH, 0, AUX_SC1);
  MEMBAR();
  SB();
  const rsrc_t rv = mk(vc + (size_t)rid * 32768, 65536);
  const int fb = (j >> 3) * 8 + (bid & 7);
  const bool ffn = fb < FF / 64;
  const int pc = ffn ? fb : 0;
  const rsrc_t ro = mk(wo + (size_t)h * DH * DM, DH * DM * 2);
  const rsrc_t rd = mk(wd + (size_t)(2 * pc + s) * 32 * DM, ffn ? 32 * DM * 2 : 0);
  const int xoff = WS_X + ((h * NB + b) * 8 + w) * 1024;
  const bool xuse = ffn || w < 4;
  const int krow = 160 * b + 40 * (w & 3) + (lane >> 3);
  const int pitch = w < 4 ? NH * DH * 2 : FF * 2, col = w < 4 ? h * DH + s * 64 : pc * 64;
  const rsrc_t rw = mk((w < 4 ? wq : s ? wu : wg) + col, xuse ? (unsigned)(DM * pitch - col * 2) : 0u);
  const int vw = krow * pitch + (lane & 7) * 16;
  unsigned lw[4] = {0u, 0u, 0u, 0u};
  auto issue = [&](auto I) {
    constexpr int n = decltype(I)::value;
    if constexpr (gated(n)) {
      constexpr int due = due_at(n);
      gate<due>(tbase);
    }
    constexpr int m = ord(n & 63);
    if constexpr (n < 64) kv[n] = ld16<AUX_K>(rk, voffs[m & 3], rhi ^ ((m >> 2) * 4096));
    else kv[n] = ld16<AUX_V>(rv, voffs[m & 3], rhi ^ ((m >> 2) * 4096));
    SB();
    constexpr int id = slot_at(n, D0, 25);
    if constexpr (n >= 2 && n < 7) wb[n - 2] = ld16<AUX_NT>(rw, vw, (n - 2) * 8 * pitch);
    if constexpr (n == 7 || n == 8) lw[n - 7] = __builtin_amdgcn_raw_buffer_load_b32(ro, lane < 10 ? lane * 128 : OOB, (((j & 7) * 8 + w) * 2 + n - 7) * 1280, AUX_W);
    if constexpr (n == 9) lw[2] = __builtin_amdgcn_raw_buffer_load_b32(rd, lane < 10 ? lane * 128 : OOB, (b * 8 + w) * 1280, AUX_W);
    if constexpr (SW && n == 12) lw[3] = __builtin_amdgcn_raw_buffer_load_b32(rws, lane < 5 ? lane * 128 : OOB, WS_SLAB + bid * (DM * 8) + w * 640, SW == 1 ? 0 : AUX_SC1);
    if constexpr (n == XP) {
      MEMBAR();
      rx4 = ld16<AUX_SC1>(rws, xuse ? lane * 16 : OOB, xoff);
    }
    if constexpr (id >= 0) wst[id] = ld16<AUX_W>(id < 5 ? rd : ro, tid * 16, (id < 5 ? id : id - 5) * 8192);
    SB();
  };
  static_for<2, 4>(issue);
  if (w < 4) {
    float xv[10], sum = 0.f;
#pragma unroll
    for (int i = 0; i < 5; ++i) { xv[2 * i] = lo_f(xr[i]); xv[2 * i + 1] = hi_f(xr[i]); sum += xv[2 * i] + xv[2 * i + 1]; }
    const float mean = wave_sum(sum) * (1.f / DM);
    float sq = 0.f;
#pragma unroll
    for (int i = 0; i < 10; ++i) { const float d = xv[i] - mean; sq = fmaf(d, d, sq); }
    const float var = bfr(wave_sum(sq) * (1.f / DM));
    const float mb = bfr(mean), rs = 1.f / bfr(sqrtf(bfr(var + 1e-5f)));
#pragma unroll
    for (int i = 0; i < 10; ++i) lds[L_XN + (2 * lane + 128 * (i >> 1) + (i & 1)) * 4 + w] = bfr(bfr(xv[i] - mb) * rs);
  }
  SB();
  static_for<4, 6>(issue);
  __syncthreads();
  asm volatile("" :: "v"(cw[0]), "v"(cw[1]), "v"(cw[2]));
  const unsigned tag0 = epoch + 1u, tag1 = (epoch + 1u) * 0x9E3779B1u + 0x7F4A7C15u;
  {
    Acc8 a[4];
#pragma unroll
    for (int bb = 0; bb < 4; ++bb) a[bb].zero();
    static_for<0, 5>([&](auto T) {
      constexpr int t = decltype(T)::value;
      const f32x4 xn4 = *(const f32x4*)(lds + L_XN + (krow + 8 * t) * 4);
#pragma unroll
      for (int bb = 0; bb < 4; ++bb) { fma8(a[bb], xn4[bb], wb[t]); a[bb].pin(); }
      SB();
      if constexpr (t >= 1 && t < 4) issue(IC<5 + t>{});
    });
    float v[8];
#pragma unroll
    for (int k = 0; k < 8; ++k) { v[k] = fold16(fold32(a[0][k], a[2][k]), fold32(a[1][k], a[3][k])); v[k] += dpp<ROR8>(v[k]); }
    const bool up = lane & 8;
    *(f32x4*)(lds + L_PP + w * 256 + row * 64 + (lane & 7) * 8 + (up ? 4 : 0)) = f32x4{up ? v[4] : v[0], up ? v[5] : v[1], up ? v[6] : v[2], up ? v[7] : v[3]};
  }
  SB();
  issue(IC<9>{});
  __syncthreads();
  if (w < 4) {
    f32x2 v = {0.f, 0.f};
#pragma unroll
    for (int i = 0; i < 4; ++i) v += *(const f32x2*)(lds + L_PP + ((tid >> 7) * 4 + i) * 256 + 2 * (tid & 127));
    const u32x4 r = {__float_as_uint(v[0]), tag0, __float_as_uint(v[1]), tag1};
    const int o = (((tid >> 5) & 3) * 8 + (tid >> 7) * 4 + b) * 1024 + s * 512 + (tid & 31) * 16;
    __builtin_amdgcn_raw_buffer_store_b128(r, rws, (ffn || w < 2) ? o : OOB, WS_X + h * NB * 8192, AUX_SC1);
  } else if (w == 7) {
    __builtin_amdgcn_raw_buffer_store_b64(u32x2{tbase, tag0}, rws, lane == 0 ? (b * 64 + pidx) * 8 : OOB, WS_T0, AUX_SC1);
  }
  SB();
  STAMP(7);
  static_for<10, PRE - 2>(issue);
  asm volatile("" :: "v"(lw[0]), "v"(lw[1]), "v"(lw[2]), "v"(lw[3]));
  if (__builtin_expect(xuse && __builtin_amdgcn_ballot_w64(((rx4[1] ^ tag0) | (rx4[3] ^ tag1)) != 0u) != 0ull, 0)) {
    unsigned spin = 0;
    do {
      MEMBAR();
      rx4 = ld16<AUX_SC1>(rws, lane * 16, xoff);
      __builtin_amdgcn_s_waitcnt(0x0F70);
    } while (__builtin_amdgcn_ballot_w64(((rx4[1] ^ tag0) | (rx4[3] ^ tag1)) != 0u) != 0ull && ++spin < SPIN_CAP);
  }
  *(f32x2*)(lds + L_QP + w * 128 + 2 * lane) = f32x2{__uint_as_float(rx4[0]), __uint_as_float(rx4[2])};
  SB();
  static_for<PRE - 2, PRE>(issue);
  __syncthreads();
  u32x4 q;
  {
    f32x4 s0 = {0.f, 0.f, 0.f, 0.f}, s1 = {0.f, 0.f, 0.f, 0.f};
    float gu = 0.f;
#pragma unroll
    for (int kq = 0; kq < 4; ++kq) {
      s0 += *(const f32x4*)(lds + L_QP + kq * 128 + 8 * c); s1 += *(const f32x4*)(lds + L_QP + kq * 128 + 8 * c + 4);
      gu += lds[L_QP + (4 + kq) * 128 + (lane >> 5) * 64 + 32 * s + (lane & 31)];
    }
    q[0] = pk_bf16(s0[0], s0[1]); q[1] = pk_bf16(s0[2], s0[3]); q[2] = pk_bf16(s1[0], s1[1]); q[3] = pk_bf16(s1[2], s1[3]);
    gu = bfr(gu);
    auto sw = __builtin_amdgcn_permlane32_swap(__float_as_uint(gu), __float_as_uint(gu), false, false);
    const float hv = bfr(__uint_as_float(sw[0]) * __uint_as_float(sw[1]));
    if (lane < 32) ((u16*)(lds + L_HH + w * 16))[lane] = (u16)(__float_as_uint(hv) >> 16);
    WAVE_SYNC();
  }
  STAMP(1);

  f32x4 y[5];
  u32x4 bfrag[4][5];
  auto mfma = [](u32x4 a, u32x4 bb, f32x4 cacc) { return __builtin_amdgcn_mfma_f32_16x16x32_bf16(__builtin_bit_cast(bf16x8, a), __builtin_bit_cast(bf16x8, bb), cacc, 0, 0, 0); };
  auto side = [&](auto U) {
    constexpr int u = decltype(U)::value;
    constexpr int iw = slot_at(u - 1, D0, 25), ir = slot_at(u - 2, D0, 25);
    if constexpr (iw >= 0) {
      const int qq = (iw % 5) * 512 + tid;
      *(u32x4*)(lds + L_ST + ((iw / 5) & 1) * 32 * STP + (qq / 80) * STP + (qq % 80) * 4) = wst[iw];
      if constexpr (iw % 5 == 4) __syncthreads();
      SB();
    }
    if constexpr (ir >= 0 && ir % 5 == 4) {
      const __attribute__((address_space(3))) char* tp = (const __attribute__((address_space(3))) char*)(lds + L_ST + ((ir / 5) & 1) * 32 * STP) + (8 * row + (c >> 2)) * (STP * 4) + w * 160 + (c & 3) * 8;
      u32x4 f[5];
#pragma unroll
      for (int t = 0; t < 5; ++t) {
        const u32x2 lo = __builtin_bit_cast(u32x2, __builtin_amdgcn_ds_read_tr16_b64_v4i16((__attribute__((address_space(3))) s16x4*)(tp + t * 32)));
        const u32x2 hi = __builtin_bit_cast(u32x2, __builtin_amdgcn_ds_read_tr16_b64_v4i16((__attribute__((address_space(3))) s16x4*)(tp + 4 * STP * 4 + t * 32)));
        f[t] = u32x4{lo[0], lo[1], hi[0], hi[1]};
      }
      if constexpr (ir == 4) {
        u32x4 ha = *(const u32x4*)(lds + L_HH + w * 16 + row * 4);
        if (lane & 7) ha = u32x4{0u, 0u, 0u, 0u};
#pragma unroll
        for (int t = 0; t < 5; ++t) { y[t] = mfma(ha, f[t], f32x4{0.f, 0.f, 0.f, 0.f}); asm volatile("" : "+v"(y[t])); }
      } else {
#pragma unroll
        for (int t = 0; t < 5; ++t) { bfrag[ir / 5 - 1][t] = f[t]; asm volatile("" : "+v"(bfrag[ir / 5 - 1][t])); }
      }
      SB();
    }
  };

  constexpr float SCALE = 0.08838834764831845f, LOG2E = 1.4426950408889634f;
  float sc[4];
  {
    float xx[16];
    static_for<0, 64>([&](auto I) {
      constexpr int st = decltype(I)::value;
      float d = dot2(kv[st][0], q[0], 0.f);
      d = dot2(kv[st][1], q[1], d); d = dot2(kv[st][2], q[2], d); d = dot2(kv[st][3], q[3], d);
      asm volatile("" : "+v"(d));
      xx[st & 15] = d;
      SB();
      static_for<target(st - 1), target(st)>(issue);
      side(I);
      if constexpr ((st & 15) == 15) { sc[st >> 4] = bfr(bfr(transpose_reduce(xx, c)) * SCALE); SB(); }
    });
  }
  LSTAMP(2);
  float mw = wave_max(fmaxf(fmaxf(sc[0], sc[1]), fmaxf(sc[2], sc[3])));
  asm volatile("" : "+v"(mw));
  SB();
  static_for<target(63), target(64)>(issue);
  float p[4];
#pragma unroll
  for (int g = 0; g < 4; ++g) p[g] = __builtin_amdgcn_exp2f((sc[g] - mw) * LOG2E);
  const float lsum = wave_sum((p[0] + p[1]) + (p[2] + p[3]));
  const int rec = WS_REC + (b * NH + h) * 256;
  {
    const u32x4 r = {__float_as_uint(mw), tag0, __float_as_uint(lsum), tag1};
    __builtin_amdgcn_raw_buffer_store_b128(r, rws, lane == 0 ? (s * 8 + w) * 16 : OOB, rec, AUX_SC1);
  }
  SB();

  Acc8 acc; acc.zero();
  const bool red = w == 7 && bid >= RB0 && bid < RB0 + 160;
  const int rb = bid & 3, cg = (bid - RB0) >> 2;
  u32x4 r1;
  u32x2 rt;
  unsigned xo = 0;
  int tlag = 0;
  const bool odd = lane & 1;
  int so_ = (odd && row) ? OOB : (5 * w + (odd ? 4 : row)) * 8192 + (c >> 1) * 16;
  asm volatile("" : "+v"(so_));
  float fw = 0.f;
  static_for<0, 64>([&](auto I) {
    constexpr int st = decltype(I)::value;
    fma8(acc, dpp<BCAST + (st & 15)>(p[st >> 4]), kv[64 + st]);
    acc.pin();
    SB();
    if constexpr (st > 0) static_for<target(63 + st), target(64 + st)>(issue);
    side(IC<64 + st>{});
    if constexpr (st == RLD) { MEMBAR(); r1 = ld16<AUX_SC1>(rws, c * 16, rec); SB(); }
    if constexpr (st == RLD + 2) { xo = __builtin_amdgcn_raw_buffer_load_b16(mk(x + rb * DM, red ? DM * 2 : 0), (cg * 16 + 2 * (lane & 7) + (lane >> 5)) * 2, 0, AUX_W); SB(); }
    if constexpr (st == RLD - 2) { MEMBAR(); rt = __builtin_amdgcn_raw_buffer_load_b64(rws, red ? lane * 8 : OOB, WS_T0 + rb * 512, AUX_SC1); SB(); }
    if constexpr (st == RLD + RING) {
      const int dt = (int)(rt[0] - tbase);
      tlag = (int)wave_max(rt[1] == tag0 && dt < 150 ? (float)dt : -1000.f);
      asm volatile("" : "+v"(tlag));
      SB();
    }
    if constexpr (st == RLD + RING + 2) {
      if (__builtin_expect(__builtin_amdgcn_ballot_w64(((r1[1] ^ tag0) | (r1[3] ^ tag1)) != 0u) != 0ull, 0)) {
        unsigned spin = 0;
        do {
          __builtin_amdgcn_s_sleep(8); MEMBAR();
          r1 = ld16<AUX_SC1>(rws, c * 16, rec);
          __builtin_amdgcn_s_waitcnt(0x0F70);
        } while (__builtin_amdgcn_ballot_w64(((r1[1] ^ tag0) | (r1[3] ^ tag1)) != 0u) != 0ull && ++spin < SPIN_CAP);
      }
      const float mi = __uint_as_float(r1[0]), li = __uint_as_float(r1[2]);
      const float M = row_max(mi);
      const float L = row_sum(li * __builtin_amdgcn_exp2f((mi - M) * LOG2E));
      fw = __builtin_amdgcn_exp2f((mw - M) * LOG2E) / L;
      asm volatile("" : "+v"(fw));
      SB();
    }
  });
  LSTAMP(3);
  {
    const float e0 = fold32(acc[0], acc[4]), e1 = fold32(acc[1], acc[5]), e2 = fold32(acc[2], acc[6]), e3 = fold32(acc[3], acc[7]);
    ((unsigned*)lds)[L_OP + w * 72 + 4 * c + row] = pk_bf16(fold16(e0, e2) * fw, fold16(e1, e3) * fw);
  }
  __syncthreads();
  {
    u32x4 af[4];
#pragma unroll
    for (int ks = 0; ks < 4; ++ks) af[ks] = *(const u32x4*)(lds + L_OP + (lane & 7) * 72 + ks * 16 + row * 4);
#pragma unroll
    for (int ks = 0; ks < 4; ++ks)
#pragma unroll
      for (int t = 0; t < 5; ++t) y[t] = mfma(af[ks], bfrag[ks][t], y[t]);
    float sm[5];
#pragma unroll
    for (int t = 0; t < 5; ++t) sm[t] = (y[t][0] + y[t][1]) + (y[t][2] + y[t][3]);
    const float v01 = fold16(sm[0], sm[1]), v23 = fold16(sm[2], sm[3]), v4 = fold16(sm[4], sm[4]);
    const float v = lane < 32 ? v01 : v23;
    const float vn = dpp<XOR1>(v), v4n = dpp<XOR1>(v4);
    __builtin_amdgcn_raw_buffer_store_b128(u32x4{__float_as_uint(odd ? v4n : v), tag0, __float_as_uint(odd ? v4 : vn), tag1}, rws,
                                           so_, WS_SLAB + b * 64 * DM * 8 + pidx * 128, AUX_SC1);
  }
  LSTAMP(4);

  if (red) {
    const int so = WS_SLAB + rb * 64 * DM * 8 + cg * 8192, vo = lane * 16;
    auto poll = [&](u32x4 (&t)[8]) {
      MEMBAR();
#pragma unroll
      for (int i = 0; i < 8; ++i) { t[i] = ld16<AUX_SC1>(rws, vo + (i & 3) * 1024, so + (i >> 2) * 4096); SB(); }
    };
    f32x2 f;
    auto take = [&](u32x4 (&t)[8]) -> bool {
      unsigned bad = 0;
      f = f32x2{0.f, 0.f};
#pragma unroll
      for (int i = 0; i < 8; ++i) {
        bad |= (t[i][1] ^ tag0) | (t[i][3] ^ tag1); f[0] += __uint_as_float(t[i][0]); f[1] += __uint_as_float(t[i][2]);
        asm volatile("" : "+v"(bad), "+v"(f));
      }
      return __builtin_amdgcn_ballot_w64(bad != 0u) == 0ull;
    };
    u32x4 ta[8];
    gate<due_at(126) + PD0>(tbase + (unsigned)__builtin_amdgcn_readfirstlane(tlag));
    for (unsigned spin = 0; spin < SPIN_CAP; ++spin) { poll(ta); if (take(ta)) break; }
    float v = fold32(f[0], f[1]);
    v = fold16(v, v); v += dpp<ROR8>(v);
    __builtin_amdgcn_raw_buffer_store_b16(__builtin_bit_cast(u16, (__bf16)(lo_f(xo) + v)), mk(out, NB * DM * 2),
                                          (lane & 24) == 0 ? (rb * DM + cg * 16 + 2 * (lane & 7) + (lane >> 5)) * 2 : OOB, 0, 0);
    if (lane == 0 && bid == RB0) __builtin_amdgcn_raw_buffer_store_b32(tag0, rws, WS_EPOCH, 0, AUX_SC1);
    STAMP(5);
  }
  STAMP_OUT();
  asm volatile(".Lfd_code1:");
}

}

extern "C" void kernel_launch(void* const* d_in, const int* in_sizes, int n_in, void* d_out, int out_size, void* d_ws, size_t ws_size,
                              hipStream_t stream) {
  (void)in_sizes; (void)n_in; (void)out_size; (void)ws_size;
  fused_decode<<<NWG, NTHR, 0, stream>>>((const u16*)d_in[0], (const u16*)d_in[1], (const u16*)d_in[2], (const u16*)d_in[3],
                                         (const u16*)d_in[4], (const u16*)d_in[6], (const u16*)d_in[7], (const u16*)d_in[8],
                                         (u16*)d_out, (unsigned char*)d_ws);
}
```

```cpp
#include <hip/hip_runtime.h>
#include <hip/hip_bf16.h>
#include <stdint.h>

namespace {

constexpr int NB = 4, DM = 640, NH = 32, SK = 4096, DH = 128, FF = 1728;
constexpr int NWG = 256, NTHR = 512;
#define TAU_NS 270.
#define CW 1.15
#define CL 1.15
#define CP .6
#define CX 1.15
#define SLACK0 1.
#define SLACKM 0.25
#define RING 8
#define RLD 40
#define CATCH 4
#define PAT 3
#define PD0 145
#define PD1 6
#define SW 0
#define SYNC 0
#define T0ALIGN ""
#define RB0 0
#define KWARM 1
#define PSK 0
#define AUXW 0

typedef unsigned short u16;
typedef unsigned u32x4 __attribute__((ext_vector_type(4)));
typedef unsigned u32x2 __attribute__((ext_vector_type(2)));
typedef float f32x4 __attribute__((ext_vector_type(4)));
typedef float f32x2 __attribute__((ext_vector_type(2)));
typedef __bf16 bf16x2 __attribute__((ext_vector_type(2)));
typedef short s16x4 __attribute__((ext_vector_type(4)));
typedef __bf16 bf16x8 __attribute__((ext_vector_type(8)));
typedef __amdgpu_buffer_rsrc_t rsrc_t;

constexpr int AUX_K = 2, AUX_V = 2, AUX_W = AUXW, AUX_NT = 2, AUX_SC1 = 16, RSRC_W3 = 0x00020000;

constexpr int WS_EPOCH = 0, WS_T0 = 2048, WS_REC = 4096, WS_SLAB = WS_REC + NB * NH * 16 * 16, WS_X = WS_SLAB + NB * 64 * DM * 8, WS_END = WS_X + NH * NB * 8192;
constexpr int WS_STAMP = 4 << 20;
constexpr unsigned SPIN_CAP = 1u << 18;
constexpr int OOB = 1 << 28;

constexpr int L_XN = 0;
constexpr int L_PP = L_XN + 640 * 4;
constexpr int L_QP = L_PP + 8 * 256;
constexpr int L_HH = L_QP + 8 * 128;
constexpr int L_OP = L_HH + 8 * 16;
constexpr int L_ST = L_OP + 8 * 72;
constexpr int STP = 328;
constexpr int L_END = L_ST + 2 * 32 * STP;

__device__ __forceinline__ rsrc_t mk(const void* p, unsigned bytes) {
  return __builtin_amdgcn_make_buffer_rsrc(const_cast<void*>(p), 0, (int)bytes, RSRC_W3);
}
template <int AUX> __device__ __forceinline__ u32x4 ld16(rsrc_t r, int voff, int soff) {
  return __builtin_amdgcn_raw_buffer_load_b128(r, voff, soff, AUX);
}
__device__ __forceinline__ float lo_f(unsigned u) { return __builtin_bit_cast(float, u << 16); }
__device__ __forceinline__ float hi_f(unsigned u) { return __builtin_bit_cast(float, u & 0xffff0000u); }
__device__ __forceinline__ float bfr(float f) { return (float)(__bf16)f; }
__device__ __forceinline__ unsigned pk_bf16(float lo, float hi) {
  bf16x2 v = {(__bf16)lo, (__bf16)hi};
  return __builtin_bit_cast(unsigned, v);
}
__device__ __forceinline__ float dot2(unsigned a, unsigned b, float acc) {
  return __builtin_amdgcn_fdot2_f32_bf16(__builtin_bit_cast(bf16x2, a), __builtin_bit_cast(bf16x2, b), acc, false);
}
template <int CTRL> __device__ __forceinline__ float dpp(float x) {
  return __builtin_bit_cast(float, __builtin_amdgcn_mov_dpp(__builtin_bit_cast(int, x), CTRL, 0xf, 0xf, true));
}
constexpr int XOR1 = 0xB1, XOR2 = 0x4E, XOR7 = 0x141, ROR8 = 0x128, ROR4 = 0x124, BCAST = 0x150;
__device__ __forceinline__ float fold32(float a, float b) {
  auto s = __builtin_amdgcn_permlane32_swap(__builtin_bit_cast(unsigned, a), __builtin_bit_cast(unsigned, b), false, false);
  return __builtin_bit_cast(float, (unsigned)s[0]) + __builtin_bit_cast(float, (unsigned)s[1]);
}
__device__ __forceinline__ float fold16(float a, float b) {
  auto s = __builtin_amdgcn_permlane16_swap(__builtin_bit_cast(unsigned, a), __builtin_bit_cast(unsigned, b), false, false);
  return __builtin_bit_cast(float, (unsigned)s[0]) + __builtin_bit_cast(float, (unsigned)s[1]);
}
__device__ __forceinline__ float row_sum(float v) {
  v += dpp<XOR1>(v); v += dpp<XOR2>(v); v += dpp<XOR7>(v); v += dpp<ROR8>(v); return v;
}
__device__ __forceinline__ float row_max(float v) {
  v = fmaxf(v, dpp<XOR1>(v)); v = fmaxf(v, dpp<XOR2>(v)); v = fmaxf(v, dpp<XOR7>(v)); v = fmaxf(v, dpp<ROR8>(v)); return v;
}
__device__ __forceinline__ float wave_sum(float v) { v = row_sum(v); v = fold16(v, v); return fold32(v, v); }
__device__ __forceinline__ float wave_max(float v) {
  v = row_max(v);
  auto s = __builtin_amdgcn_permlane16_swap(__builtin_bit_cast(unsigned, v), __builtin_bit_cast(unsigned, v), false, false);
  v = fmaxf(__builtin_bit_cast(float, (unsigned)s[0]), __builtin_bit_cast(float, (unsigned)s[1]));
  auto t = __builtin_amdgcn_permlane32_swap(__builtin_bit_cast(unsigned, v), __builtin_bit_cast(unsigned, v), false, false);
  return fmaxf(__builtin_bit_cast(float, (unsigned)t[0]), __builtin_bit_cast(float, (unsigned)t[1]));
}
struct Acc8 {
  f32x2 v[4];
  __device__ __forceinline__ void zero() { v[0] = v[1] = v[2] = v[3] = f32x2{0.f, 0.f}; }
  __device__ __forceinline__ float operator[](int i) const { return v[i >> 1][i & 1]; }
  __device__ __forceinline__ void pin() { asm volatile("" : "+v"(v[0]), "+v"(v[1]), "+v"(v[2]), "+v"(v[3])); }
};
__device__ __forceinline__ void fma8(Acc8& acc, float s, u32x4 v) {
  const f32x2 ss = {s, s};
#pragma unroll
  for (int k = 0; k < 4; ++k) acc.v[k] = __builtin_elementwise_fma(ss, f32x2{lo_f(v[k]), hi_f(v[k])}, acc.v[k]);
}
#define MEMBAR() asm volatile("" ::: "memory")
#define SB() __builtin_amdgcn_sched_barrier(0)
#define WAVE_SYNC() do { __builtin_amdgcn_fence(__ATOMIC_RELEASE, "wavefront"); __builtin_amdgcn_wave_barrier(); __builtin_amdgcn_fence(__ATOMIC_ACQUIRE, "wavefront"); } while (0)

#define STAMP(i) do {} while (0)
#define STAMP_OUT() do {} while (0)

#define ESTAMP(i) do {} while (0)
#define LSTAMP(i) STAMP(i)
template <int I> struct IC { static constexpr int value = I; };
template <int I, int N, class F> __device__ __forceinline__ void static_for(F&& f) {
  if constexpr (I < N) { f(IC<I>{}); static_for<I + 1, N>(f); }
}
__device__ __forceinline__ float transpose_reduce(float (&x)[16], int c) {
#define TR_STEP(HALF, CTRL) _Pragma("unroll") for (int i = 0; i < (HALF); ++i) { \
    const float lo_ = x[i], hi_ = x[i + (HALF)]; const float t1_ = lo_ + dpp<CTRL>(lo_), t2_ = hi_ + dpp<CTRL>(hi_); \
    x[i] = (c & (HALF)) ? t2_ : t1_; }
  TR_STEP(8, ROR8) TR_STEP(4, XOR7) TR_STEP(2, XOR2) TR_STEP(1, XOR1)
#undef TR_STEP
  return x[0];
}

constexpr int XP = 15, PRE = 24, D0 = PRE + 2;
static_assert(D0 + 3 * 24 + 4 < 124 && !(RING & 1), "wo lands before its use");
constexpr int slot_at(int n, int first, int cnt) { return (n >= first && (n - first) % 3 == 0 && (n - first) / 3 < cnt) ? (n - first) / 3 : -1; }
constexpr double TAU = TAU_NS / 10.;
constexpr bool gated(int n) { return (n >= 2 && n <= 11) || (n > 11 && n <= XP ? (n & 1) != 0 : (n & 1) == 0); }
constexpr int gate_no(int n) { int g = 0; for (int m = 0; m < n; ++m) g += gated(m); return g; }
constexpr int EXTRA[18] = {0, 2, 2, 2, 2, 2, 10, 10, 2, -11, 4, 0, -9, 0, 0, 0, 0, 4};
constexpr int due_at(int n) { double t = 0; for (int g = 0; g < gate_no(n); ++g) t += 2 * TAU + (g < 18 ? EXTRA[g] : g == 41 ? 4 : 0); return (int)t; }
constexpr int target(int u) {
  const int drip = PRE + 2 * ((u + 2 * CATCH) / (2 * CATCH)), mid = u >= 64 ? 64 + RING + 2 : 0;
  int t = (u + 1 + RING) & ~1;
  t = t < drip ? drip : t; t = t < mid ? mid : t;
  return u < 0 ? PRE : (t > 128 ? 128 : t);
}
constexpr int RCOL[12] = {33, 62, 8, 25, 32, 9, 36, 28, 40, 48, 41, 0}, OCOL[6] = {33, 21, 1, 16, 57, 18};
constexpr int ord(int n) { int r = 0; for (int k = 0; k < 6; ++k) if ((n >> k) & 1) r ^= OCOL[k]; return r; }
constexpr unsigned long long rtab(int k0) { unsigned long long t = 0; for (int i = 0; i < 8; ++i) { int r = 0; for (int k = 0; k < 3; ++k) if ((i >> k) & 1) r ^= RCOL[k0 + k]; t |= (unsigned long long)r << (8 * i); } return t; }
template <int DUE> __device__ __forceinline__ void gate(unsigned base) {
  asm volatile("1:\n\ts_memrealtime vcc\n\ts_waitcnt lgkmcnt(0)\n\ts_sub_u32 vcc_lo, vcc_lo, %0\n\ts_cmp_ge_i32 vcc_lo, %1\n\ts_cbranch_scc1 2f\n\ts_sleep 1\n\ts_branch 1b\n2:"
               :: "s"(base), "n"(DUE) : "vcc", "scc");
}

__global__ void __launch_bounds__(NTHR, 2)
fused_decode(const u16* x, const u16* __restrict__ kc, const u16* __restrict__ vc, const u16* __restrict__ wq,
             const u16* __restrict__ wo, const u16* __restrict__ wg, const u16* __restrict__ wu, const u16* __restrict__ wd,
             u16* __restrict__ out, unsigned char* ws) {
  __shared__ __attribute__((aligned(16))) float lds[L_END];
  SB();
  const int tid = threadIdx.x, lane = tid & 63, w = __builtin_amdgcn_readfirstlane(tid >> 6);
  const int bid = blockIdx.x, j = bid >> 3;
  const int h = (bid & 7) * 4 + (j >> 3), b = (j & 7) >> 1, s = j & 1;
  const int pidx = 2 * h + s;
  const int row = lane >> 4, c = lane & 15;
  const unsigned rid = (unsigned)(((b * NH + h) * 2 + s) * 8 + w);
  const unsigned rot = (unsigned)((rtab(0) >> (8 * (rid & 7))) ^ (rtab(3) >> (8 * ((rid >> 3) & 7))) ^ (rtab(6) >> (8 * ((rid >> 6) & 7))) ^ (rtab(9) >> (8 * (rid >> 9)))) & 63u;
  int voffs[4];
#pragma unroll
  for (int k = 0; k < 4; ++k) voffs[k] = lane * 16 + (int)((k ^ rot) & 3u) * 1024;
  const int rhi = (int)(rot >> 2) * 4096;
  u32x4 kv[128], wb[5], wst[25], rx4;
  unsigned tbase;
  {
    const unsigned pord = ((unsigned)bid & 0x3Eu) | (((unsigned)bid & 1u) << 6) | (((unsigned)bid & 64u) << 1) | ((unsigned)bid >> 7);
    const unsigned phi = (pord * (unsigned)(2 * TAU + .5)) >> 8;
    const int r1_ = rhi ^ ((ord(1) >> 2) * 4096);
    asm volatile("" :: "s"(phi), "s"(rhi), "s"(r1_), "v"(voffs[0]), "v"(voffs[ord(1) & 3]));
    SB();
    asm volatile(T0ALIGN "s_memrealtime vcc\n\ts_waitcnt lgkmcnt(0)\n\ts_add_u32 %0, vcc_lo, %1" : "=s"(tbase) : "s"(phi), "s"(kc), "s"(wd), "s"(ws), "s"(rhi), "s"(r1_), "v"(voffs[0]), "v"(voffs[ord(1) & 3]) : "vcc", "scc");
  }
  const rsrc_t rk = mk(kc + (size_t)rid * 32768, 65536);
  gate<0>(tbase);
  kv[0] = ld16<AUX_K>(rk, voffs[0], rhi); kv[1] = ld16<AUX_K>(rk, voffs[ord(1) & 3], rhi ^ ((ord(1) >> 2) * 4096));
  asm volatile("" : "+s"(x), "+s"(vc), "+s"(wq), "+s"(wo), "+s"(wg), "+s"(wu), "+s"(wd), "+s"(ws));
  SB();
  STAMP(6);
  unsigned xr[5];
  {
    const rsrc_t rxw = mk(x + (w & 3) * DM, w < 4 ? DM * 2 : 0);
#pragma unroll
    for (int i = 0; i < 5; ++i) xr[i] = __builtin_amdgcn_raw_buffer_load_b32(rxw, lane * 4 + i * 256, 0, AUX_W);
  }
  unsigned cw[3];
  {
    unsigned long long pc; unsigned len;
    asm volatile("s_getpc_b64 %0\n.Lfd_code0:\n\ts_mov_b32 %1, .Lfd_code1-.Lfd_code0" : "=s"(pc), "=s"(len));
    const rsrc_t rc = mk((const void*)pc, (j & 15) == 0 ? len : 0u);
#pragma unroll
    for (int i = 0; i < 3; ++i) cw[i] = __builtin_amdgcn_raw_buffer_load_b32(rc, w == 5 ? lane * 128 : OOB, i * 8192, 0);
  }
  const rsrc_t rws = mk(ws, WS_END);
  const unsigned epoch = __builtin_amdgcn_raw_buffer_load_b32(rws, WS_EPOCH, 0, AUX_SC1);
  MEMBAR();
  SB();
  const rsrc_t rv = mk(vc + (size_t)rid * 32768, 65536);
  const int fb = (j >> 3) * 8 + (bid & 7);
  const bool ffn = fb < FF / 64;
  const int pc = ffn ? fb : 0;
  const rsrc_t ro = mk(wo + (size_t)h * DH * DM, DH * DM * 2);
  const rsrc_t rd = mk(wd + (size_t)(2 * pc + s) * 32 * DM, ffn ? 32 * DM * 2 : 0);
  const int xoff = WS_X + ((h * NB + b) * 8 + w) * 1024;
  const bool xuse = ffn || w < 4;
  const int krow = 160 * b + 40 * (w & 3) + (lane >> 3);
  const int pitch = w < 4 ? NH * DH * 2 : FF * 2, col = w < 4 ? h * DH + s * 64 : pc * 64;
  const rsrc_t rw = mk((w < 4 ? wq : s ? wu : wg) + col, xuse ? (unsigned)(DM * pitch - col * 2) : 0u);
  const int vw = krow * pitch + (lane & 7) * 16;
  unsigned lw[4] = {0u, 0u, 0u, 0u};
  auto issue = [&](auto I) {
    constexpr int n = decltype(I)::value;
    if constexpr (gated(n)) {
      constexpr int due = due_at(n);
      gate<due>(tbase);
    }
    constexpr int m = ord(n & 63);
    if constexpr (n < 64) kv[n] = ld16<AUX_K>(rk, voffs[m & 3], rhi ^ ((m >> 2) * 4096));
    else kv[n] = ld16<AUX_V>(rv, voffs[m & 3], rhi ^ ((m >> 2) * 4096));
    SB();
    constexpr int id = slot_at(n, D0, 25);
    if constexpr (n >= 2 && n < 7) wb[n - 2] = ld16<AUX_NT>(rw, vw, (n - 2) * 8 * pitch);
    if constexpr (n == 7 || n == 8) lw[n - 7] = __builtin_amdgcn_raw_buffer_load_b32(ro, lane < 10 ? lane * 128 : OOB, (((j & 7) * 8 + w) * 2 + n - 7) * 1280, AUX_W);
    if constexpr (n == 9) lw[2] = __builtin_amdgcn_raw_buffer_load_b32(rd, lane < 10 ? lane * 128 : OOB, (b * 8 + w) * 1280, AUX_W);
    if constexpr (SW && n == 12) lw[3] = __builtin_amdgcn_raw_buffer_load_b32(rws, lane < 5 ? lane * 128 : OOB, WS_SLAB + bid * (DM * 8) + w * 640, SW == 1 ? 0 : AUX_SC1);
    if constexpr (n == XP) {
      MEMBAR();
      rx4 = ld16<AUX_SC1>(rws, xuse ? lane * 16 : OOB, xoff);
    }
    if constexpr (id >= 0) wst[id] = ld16<AUX_W>(id < 5 ? rd : ro, tid * 16, (id < 5 ? id : id - 5) * 8192);
    SB();
  };
  static_for<2, 4>(issue);
  if (w < 4) {
    float xv[10], sum = 0.f;
#pragma unroll
    for (int i = 0; i < 5; ++i) { xv[2 * i] = lo_f(xr[i]); xv[2 * i + 1] = hi_f(xr[i]); sum += xv[2 * i] + xv[2 * i + 1]; }
    const float mean = wave_sum(sum) * (1.f / DM);
    float sq = 0.f;
#pragma unroll
    for (int i = 0; i < 10; ++i) { const float d = xv[i] - mean; sq = fmaf(d, d, sq); }
    const float var = bfr(wave_sum(sq) * (1.f / DM));
    const float mb = bfr(mean), rs = 1.f / bfr(sqrtf(bfr(var + 1e-5f)));
#pragma unroll
    for (int i = 0; i < 10; ++i) lds[L_XN + (2 * lane + 128 * (i >> 1) + (i & 1)) * 4 + w] = bfr(bfr(xv[i] - mb) * rs);
  }
  SB();
  static_for<4, 6>(issue);
  __syncthreads();
  asm volatile("" :: "v"(cw[0]), "v"(cw[1]), "v"(cw[2]));
  const unsigned tag0 = epoch + 1u, tag1 = (epoch + 1u) * 0x9E3779B1u + 0x7F4A7C15u;
  {
    Acc8 a[4];
#pragma unroll
    for (int bb = 0; bb < 4; ++bb) a[bb].zero();
    static_for<0, 5>([&](auto T) {
      constexpr int t = decltype(T)::value;
      const f32x4 xn4 = *(const f32x4*)(lds + L_XN + (krow + 8 * t) * 4);
#pragma unroll
      for (int bb = 0; bb < 4; ++bb) { fma8(a[bb], xn4[bb], wb[t]); a[bb].pin(); }
      SB();
      if constexpr (t >= 1 && t < 4) issue(IC<5 + t>{});
    });
    float v[8];
#pragma unroll
    for (int k = 0; k < 8; ++k) { v[k] = fold16(fold32(a[0][k], a[2][k]), fold32(a[1][k], a[3][k])); v[k] += dpp<ROR8>(v[k]); }
    const bool up = lane & 8;
    *(f32x4*)(lds + L_PP + w * 256 + row * 64 + (lane & 7) * 8 + (up ? 4 : 0)) = f32x4{up ? v[4] : v[0], up ? v[5] : v[1], up ? v[6] : v[2], up ? v[7] : v[3]};
  }
  SB();
  issue(IC<9>{});
  __syncthreads();
  if (w < 4) {
    f32x2 v = {0.f, 0.f};
#pragma unroll
    for (int i = 0; i < 4; ++i) v += *(const f32x2*)(lds + L_PP + ((tid >> 7) * 4 + i) * 256 + 2 * (tid & 127));
    const u32x4 r = {__float_as_uint(v[0]), tag0, __float_as_uint(v[1]), tag1};
    const int o = (((tid >> 5) & 3) * 8 + (tid >> 7) * 4 + b) * 1024 + s * 512 + (tid & 31) * 16;
    __builtin_amdgcn_raw_buffer_store_b128(r, rws, (ffn || w < 2) ? o : OOB, WS_X + h * NB * 8192, AUX_SC1);
  } else if (w == 7) {
    __builtin_amdgcn_raw_buffer_store_b64(u32x2{tbase, tag0}, rws, lane == 0 ? (b * 64 + pidx) * 8 : OOB, WS_T0, AUX_SC1);
  }
  SB();
  STAMP(7);
  static_for<10, PRE - 2>(issue);
  asm volatile("" :: "v"(lw[0]), "v"(lw[1]), "v"(lw[2]), "v"(lw[3]));
  if (__builtin_expect(xuse && __builtin_amdgcn_ballot_w64(((rx4[1] ^ tag0) | (rx4[3] ^ tag1)) != 0u) != 0ull, 0)) {
    unsigned spin = 0;
    do {
      MEMBAR();
      rx4 = ld16<AUX_SC1>(rws, lane * 16, xoff);
      __builtin_amdgcn_s_waitcnt(0x0F70);
    } while (__builtin_amdgcn_ballot_w64(((rx4[1] ^ tag0) | (rx4[3] ^ tag1)) != 0u) != 0ull && ++spin < SPIN_CAP);
  }
  *(f32x2*)(lds + L_QP + w * 128 + 2 * lane) = f32x2{__uint_as_float(rx4[0]), __uint_as_float(rx4[2])};
  SB();
  static_for<PRE - 2, PRE>(issue);
  __syncthreads();
  u32x4 q;
  {
    f32x4 s0 = {0.f, 0.f, 0.f, 0.f}, s1 = {0.f, 0.f, 0.f, 0.f};
    float gu = 0.f;
#pragma unroll
    for (int kq = 0; kq < 4; ++kq) {
      s0 += *(const f32x4*)(lds + L_QP + kq * 128 + 8 * c); s1 += *(const f32x4*)(lds + L_QP + kq * 128 + 8 * c + 4);
      gu += lds[L_QP + (4 + kq) * 128 + (lane >> 5) * 64 + 32 * s + (lane & 31)];
    }
    q[0] = pk_bf16(s0[0], s0[1]); q[1] = pk_bf16(s0[2], s0[3]); q[2] = pk_bf16(s1[0], s1[1]); q[3] = pk_bf16(s1[2], s1[3]);
    gu = bfr(gu);
    auto sw = __builtin_amdgcn_permlane32_swap(__float_as_uint(gu), __float_as_uint(gu), false, false);
    const float hv = bfr(__uint_as_float(sw[0]) * __uint_as_float(sw[1]));
    if (lane < 32) ((u16*)(lds + L_HH + w * 16))[lane] = (u16)(__float_as_uint(hv) >> 16);
    WAVE_SYNC();
  }
  STAMP(1);

  f32x4 y[5];
  u32x4 bfrag[4][5];
  auto mfma = [](u32x4 a, u32x4 bb, f32x4 cacc) { return __builtin_amdgcn_mfma_f32_16x16x32_bf16(__builtin_bit_cast(bf16x8, a), __builtin_bit_cast(bf16x8, bb), cacc, 0, 0, 0); };
  auto side = [&](auto U) {
    constexpr int u = decltype(U)::value;
    constexpr int iw = slot_at(u - 1, D0, 25), ir = slot_at(u - 2, D0, 25);
    if constexpr (iw >= 0) {
      const int qq = (iw % 5) * 512 + tid;
      *(u32x4*)(lds + L_ST + ((iw / 5) & 1) * 32 * STP + (qq / 80) * STP + (qq % 80) * 4) = wst[iw];
      if constexpr (iw % 5 == 4) __syncthreads();
      SB();
    }
    if constexpr (ir >= 0 && ir % 5 == 4) {
      const __attribute__((address_space(3))) char* tp = (const __attribute__((address_space(3))) char*)(lds + L_ST + ((ir / 5) & 1) * 32 * STP) + (8 * row + (c >> 2)) * (STP * 4) + w * 160 + (c & 3) * 8;
      u32x4 f[5];
#pragma unroll
      for (int t = 0; t < 5; ++t) {
        const u32x2 lo = __builtin_bit_cast(u32x2, __builtin_amdgcn_ds_read_tr16_b64_v4i16((__attribute__((address_space(3))) s16x4*)(tp + t * 32)));
        const u32x2 hi = __builtin_bit_cast(u32x2, __builtin_amdgcn_ds_read_tr16_b64_v4i16((__attribute__((address_space(3))) s16x4*)(tp + 4 * STP * 4 + t * 32)));
        f[t] = u32x4{lo[0], lo[1], hi[0], hi[1]};
      }
      if constexpr (ir == 4) {
        u32x4 ha = *(const u32x4*)(lds + L_HH + w * 16 + row * 4);
        if (lane & 7) ha = u32x4{0u, 0u, 0u, 0u};
#pragma unroll
        for (int t = 0; t < 5; ++t) { y[t] = mfma(ha, f[t], f32x4{0.f, 0.f, 0.f, 0.f}); asm volatile("" : "+v"(y[t])); }
      } else {
#pragma unroll
        for (int t = 0; t < 5; ++t) { bfrag[ir / 5 - 1][t] = f[t]; asm volatile("" : "+v"(bfrag[ir / 5 - 1][t])); }
      }
      SB();
    }
  };

  constexpr float SCALE = 0.08838834764831845f, LOG2E = 1.4426950408889634f;
  float sc[4];
  {
    float xx[16];
    static_for<0, 64>([&](auto I) {
      constexpr int st = decltype(I)::value;
      float d = dot2(kv[st][0], q[0], 0.f);
      d = dot2(kv[st][1], q[1], d); d = dot2(kv[st][2], q[2], d); d = dot2(kv[st][3], q[3], d);
      asm volatile("" : "+v"(d));
      xx[st & 15] = d;
      SB();
      static_for<target(st - 1), target(st)>(issue);
      side(I);
      if constexpr ((st & 15) == 15) { sc[st >> 4] = bfr(bfr(transpose_reduce(xx, c)) * SCALE); SB(); }
    });
  }
  LSTAMP(2);
  float mw = wave_max(fmaxf(fmaxf(sc[0], sc[1]), fmaxf(sc[2], sc[3])));
  asm volatile("" : "+v"(mw));
  SB();
  static_for<target(63), target(64)>(issue);
  float p[4];
#pragma unroll
  for (int g = 0; g < 4; ++g) p[g] = __builtin_amdgcn_exp2f((sc[g] - mw) * LOG2E);
  const float lsum = wave_sum((p[0] + p[1]) + (p[2] + p[3]));
  const int rec = WS_REC + (b * NH + h) * 256;
  {
    const u32x4 r = {__float_as_uint(mw), tag0, __float_as_uint(lsum), tag1};
    __builtin_amdgcn_raw_buffer_store_b128(r, rws, lane == 0 ? (s * 8 + w) * 16 : OOB, rec, AUX_SC1);
  }
  SB();

  Acc8 acc; acc.zero();
  const bool red = w == 7 && bid >= RB0 && bid < RB0 + 160;
  const int rb = bid & 3, cg = (bid - RB0) >> 2;
  u32x4 r1;
  u32x2 rt;
  unsigned xo = 0;
  int tlag = 0;
  const bool odd = lane & 1;
  int so_ = (odd && row) ? OOB : (5 * w + (odd ? 4 : row)) * 8192 + (c >> 1) * 16;
  asm volatile("" : "+v"(so_));
  float fw = 0.f;
  static_for<0, 64>([&](auto I) {
    constexpr int st = decltype(I)::value;
    fma8(acc, dpp<BCAST + (st & 15)>(p[st >> 4]), kv[64 + st]);
    acc.pin();
    SB();
    if constexpr (st > 0) static_for<target(63 + st), target(64 + st)>(issue);
    side(IC<64 + st>{});
    if constexpr (st == RLD) { MEMBAR(); r1 = ld16<AUX_SC1>(rws, c * 16, rec); SB(); }
    if constexpr (st == RLD + 2) { xo = __builtin_amdgcn_raw_buffer_load_b16(mk(x + rb * DM, red ? DM * 2 : 0), (cg * 16 + 2 * (lane & 7) + (lane >> 5)) * 2, 0, AUX_W); SB(); }
    if constexpr (st == RLD - 2) { MEMBAR(); rt = __builtin_amdgcn_raw_buffer_load_b64(rws, red ? lane * 8 : OOB, WS_T0 + rb * 512, AUX_SC1); SB(); }
    if constexpr (st == RLD + RING) {
      const int dt = (int)(rt[0] - tbase);
      tlag = (int)wave_max(rt[1] == tag0 && dt < 150 ? (float)dt : -1000.f);
      asm volatile("" : "+v"(tlag));
      SB();
    }
    if constexpr (st == RLD + RING + 2) {
      if (__builtin_expect(__builtin_amdgcn_ballot_w64(((r1[1] ^ tag0) | (r1[3] ^ tag1)) != 0u) != 0ull, 0)) {
        unsigned spin = 0;
        do {
          __builtin_amdgcn_s_sleep(8); MEMBAR();
          r1 = ld16<AUX_SC1>(rws, c * 16, rec);
          __builtin_amdgcn_s_waitcnt(0x0F70);
        } while (__builtin_amdgcn_ballot_w64(((r1[1] ^ tag0) | (r1[3] ^ tag1)) != 0u) != 0ull && ++spin < SPIN_CAP);
      }
      const float mi = __uint_as_float(r1[0]), li = __uint_as_float(r1[2]);
      const float M = row_max(mi);
      const float L = row_sum(li * __builtin_amdgcn_exp2f((mi - M) * LOG2E));
      fw = __builtin_amdgcn_exp2f((mw - M) * LOG2E) / L;
      asm volatile("" : "+v"(fw));
      SB();
    }
  });
  LSTAMP(3);
  {
    const float e0 = fold32(acc[0], acc[4]), e1 = fold32(acc[1], acc[5]), e2 = fold32(acc[2], acc[6]), e3 = fold32(acc[3], acc[7]);
    ((unsigned*)lds)[L_OP + w * 72 + 4 * c + row] = pk_bf16(fold16(e0, e2) * fw, fold16(e1, e3) * fw);
  }
  __syncthreads();
  {
    u32x4 af[4];
#pragma unroll
    for (int ks = 0; ks < 4; ++ks) af[ks] = *(const u32x4*)(lds + L_OP + (lane & 7) * 72 + ks * 16 + row * 4);
#pragma unroll
    for (int ks = 0; ks < 4; ++ks)
#pragma unroll
      for (int t = 0; t < 5; ++t) y[t] = mfma(af[ks], bfrag[ks][t], y[t]);
    float sm[5];
#pragma unroll
    for (int t = 0; t < 5; ++t) sm[t] = (y[t][0] + y[t][1]) + (y[t][2] + y[t][3]);
    const float v01 = fold16(sm[0], sm[1]), v23 = fold16(sm[2], sm[3]), v4 = fold16(sm[4], sm[4]);
    const float v = lane < 32 ? v01 : v23;
    const float vn = dpp<XOR1>(v), v4n = dpp<XOR1>(v4);
    __builtin_amdgcn_raw_buffer_store_b128(u32x4{__float_as_uint(odd ? v4n : v), tag0, __float_as_uint(odd ? v4 : vn), tag1}, rws,
                                           so_, WS_SLAB + b * 64 * DM * 8 + pidx * 128, AUX_SC1);
  }
  LSTAMP(4);

  if (red) {
    const int so = WS_SLAB + rb * 64 * DM * 8 + cg * 8192, vo = lane * 16;
    auto poll = [&](u32x4 (&t)[8]) {
      MEMBAR();
#pragma unroll
      for (int i = 0; i < 8; ++i) { t[i] = ld16<AUX_SC1>(rws, vo + (i & 3) * 1024, so + (i >> 2) * 4096); SB(); }
    };
    f32x2 f;
    auto take = [&](u32x4 (&t)[8]) -> bool {
      unsigned bad = 0;
      f = f32x2{0.f, 0.f};
#pragma unroll
      for (int i = 0; i < 8; ++i) {
        bad |= (t[i][1] ^ tag0) | (t[i][3] ^ tag1); f[0] += __uint_as_float(t[i][0]); f[1] += __uint_as_float(t[i][2]);
        asm volatile("" : "+v"(bad), "+v"(f));
      }
      return __builtin_amdgcn_ballot_w64(bad != 0u) == 0ull;
    };
    u32x4 ta[8];
    gate<due_at(126) + PD0>(tbase + (unsigned)__builtin_amdgcn_readfirstlane(tlag));
    for (unsigned spin = 0; spin < SPIN_CAP; ++spin) { poll(ta); if (take(ta)) break; }
    float v = fold32(f[0], f[1]);
    v = fold16(v, v); v += dpp<ROR8>(v);
    __builtin_amdgcn_raw_buffer_store_b16(__builtin_bit_cast(u16, (__bf16)(lo_f(xo) + v)), mk(out, NB * DM * 2),
                                          (lane & 24) == 0 ? (rb * DM + cg * 16 + 2 * (lane & 7) + (lane >> 5)) * 2 : OOB, 0, 0);
    if (lane == 0 && bid == RB0) __builtin_amdgcn_raw_buffer_store_b32(tag0, rws, WS_EPOCH, 0, AUX_SC1);
    STAMP(5);
  }
  if (w == 6) {
    float mn = 0.f;
    if (1) {
      for (int i = 0; i < 4; ++i) { u32x2 e = __builtin_amdgcn_raw_buffer_load_b64(rws, lane * 8, WS_T0 + i * 512, AUX_SC1); const int dt = (int)(tbase - e[0]); if (e[1] == tag0 && dt < 1000) mn = fmaxf(mn, (float)dt); }
      mn = wave_max(mn);
    }
    int back = (int)mn; asm volatile("" : "+v"(back));
    gate<8000>(tbase - (unsigned)__builtin_amdgcn_readfirstlane(back));
  }
  asm volatile(".Lfd_code1:");
}

}

extern "C" void kernel_launch(void* const* d_in, const int* in_sizes, int n_in, void* d_out, int out_size, void* d_ws, size_t ws_size,
                              hipStream_t stream) {
  (void)in_sizes; (void)n_in; (void)out_size; (void)ws_size;
  fused_decode<<<NWG, NTHR, 0, stream>>>((const u16*)d_in[0], (const u16*)d_in[1], (const u16*)d_in[2], (const u16*)d_in[3],
                                         (const u16*)d_in[4], (const u16*)d_in[6], (const u16*)d_in[7], (const u16*)d_in[8],
                                         (u16*)d_out, (unsigned char*)d_ws);
}
```

```cpp
#include <hip/hip_runtime.h>
#include <hip/hip_bf16.h>
#include <stdint.h>

namespace {

constexpr int NB = 4, DM = 640, NH = 32, DH = 128, FF = 1728;
constexpr int NWG = 256, NTHR = 512;

typedef unsigned short u16;
typedef unsigned u32x4 __attribute__((ext_vector_type(4)));
typedef unsigned u32x2 __attribute__((ext_vector_type(2)));
typedef float f32x4 __attribute__((ext_vector_type(4)));
typedef float f32x2 __attribute__((ext_vector_type(2)));
typedef __bf16 bf16x2 __attribute__((ext_vector_type(2)));
typedef short s16x4 __attribute__((ext_vector_type(4)));
typedef __bf16 bf16x8 __attribute__((ext_vector_type(8)));
typedef __amdgpu_buffer_rsrc_t rsrc_t;

constexpr int AUX_NT = 2, AUX_SC1 = 16, RSRC_W3 = 0x00020000;

constexpr int WS_EPOCH = 0, WS_T0 = 2048, WS_REC = 4096, WS_SLAB = WS_REC + NB * NH * 16 * 16, WS_X = WS_SLAB + NB * 64 * DM * 8, WS_END = WS_X + NH * NB * 8192;
constexpr unsigned SPIN_CAP = 1u << 18;
constexpr int OOB = 1 << 28;

constexpr int L_XN = 0;
constexpr int L_PP = L_XN + 640 * 4;
constexpr int L_QP = L_PP + 8 * 256;
constexpr int L_HH = L_QP + 8 * 128;
constexpr int L_OP = L_HH + 8 * 16;
constexpr int L_ST = L_OP + 8 * 72;
constexpr int STP = 328;
constexpr int L_END = L_ST + 2 * 32 * STP;

__device__ __forceinline__ rsrc_t mk(const void* p, unsigned bytes) {
  return __builtin_amdgcn_make_buffer_rsrc(const_cast<void*>(p), 0, (int)bytes, RSRC_W3);
}
template <int AUX> __device__ __forceinline__ u32x4 ld16(rsrc_t r, int voff, int soff) {
  return __builtin_amdgcn_raw_buffer_load_b128(r, voff, soff, AUX);
}
__device__ __forceinline__ float lo_f(unsigned u) { return __builtin_bit_cast(float, u << 16); }
__device__ __forceinline__ float hi_f(unsigned u) { return __builtin_bit_cast(float, u & 0xffff0000u); }
__device__ __forceinline__ float bfr(float f) { return (float)(__bf16)f; }
__device__ __forceinline__ unsigned pk_bf16(float lo, float hi) {
  bf16x2 v = {(__bf16)lo, (__bf16)hi};
  return __builtin_bit_cast(unsigned, v);
}
__device__ __forceinline__ float dot2(unsigned a, unsigned b, float acc) {
  return __builtin_amdgcn_fdot2_f32_bf16(__builtin_bit_cast(bf16x2, a), __builtin_bit_cast(bf16x2, b), acc, false);
}
template <int CTRL> __device__ __forceinline__ float dpp(float x) {
  return __builtin_bit_cast(float, __builtin_amdgcn_mov_dpp(__builtin_bit_cast(int, x), CTRL, 0xf, 0xf, true));
}
constexpr int XOR1 = 0xB1, XOR2 = 0x4E, XOR7 = 0x141, ROR8 = 0x128, BCAST = 0x150;
__device__ __forceinline__ float fold32(float a, float b) {
  auto s = __builtin_amdgcn_permlane32_swap(__builtin_bit_cast(unsigned, a), __builtin_bit_cast(unsigned, b), false, false);
  return __builtin_bit_cast(float, (unsigned)s[0]) + __builtin_bit_cast(float, (unsigned)s[1]);
}
__device__ __forceinline__ float fold16(float a, float b) {
  auto s = __builtin_amdgcn_permlane16_swap(__builtin_bit_cast(unsigned, a), __builtin_bit_cast(unsigned, b), false, false);
  return __builtin_bit_cast(float, (unsigned)s[0]) + __builtin_bit_cast(float, (unsigned)s[1]);
}
__device__ __forceinline__ float row_sum(float v) {
  v += dpp<XOR1>(v); v += dpp<XOR2>(v); v += dpp<XOR7>(v); v += dpp<ROR8>(v); return v;
}
__device__ __forceinline__ float row_max(float v) {
  v = fmaxf(v, dpp<XOR1>(v)); v = fmaxf(v, dpp<XOR2>(v)); v = fmaxf(v, dpp<XOR7>(v)); v = fmaxf(v, dpp<ROR8>(v)); return v;
}
__device__ __forceinline__ float wave_sum(float v) { v = row_sum(v); v = fold16(v, v); return fold32(v, v); }
__device__ __forceinline__ float wave_max(float v) {
  v = row_max(v);
  auto s = __builtin_amdgcn_permlane16_swap(__builtin_bit_cast(unsigned, v), __builtin_bit_cast(unsigned, v), false, false);
  v = fmaxf(__builtin_bit_cast(float, (unsigned)s[0]), __builtin_bit_cast(float, (unsigned)s[1]));
  auto t = __builtin_amdgcn_permlane32_swap(__builtin_bit_cast(unsigned, v), __builtin_bit_cast(unsigned, v), false, false);
  return fmaxf(__builtin_bit_cast(float, (unsigned)t[0]), __builtin_bit_cast(float, (unsigned)t[1]));
}
struct Acc8 {
  f32x2 v[4];
  __device__ __forceinline__ void zero() { v[0] = v[1] = v[2] = v[3] = f32x2{0.f, 0.f}; }
  __device__ __forceinline__ float operator[](int i) const { return v[i >> 1][i & 1]; }
  __device__ __forceinline__ void pin() { asm volatile("" : "+v"(v[0]), "+v"(v[1]), "+v"(v[2]), "+v"(v[3])); }
};
__device__ __forceinline__ void fma8(Acc8& acc, float s, u32x4 v) {
  const f32x2 ss = {s, s};
#pragma unroll
  for (int k = 0; k < 4; ++k) acc.v[k] = __builtin_elementwise_fma(ss, f32x2{lo_f(v[k]), hi_f(v[k])}, acc.v[k]);
}
#define MEMBAR() asm volatile("" ::: "memory")
#define SB() __builtin_amdgcn_sched_barrier(0)
#define WAVE_SYNC() do { __builtin_amdgcn_fence(__ATOMIC_RELEASE, "wavefront"); __builtin_amdgcn_wave_barrier(); __builtin_amdgcn_fence(__ATOMIC_ACQUIRE, "wavefront"); } while (0)

template <int I> struct IC { static constexpr int value = I; };
template <int I, int N, class F> __device__ __forceinline__ void static_for(F&& f) {
  if constexpr (I < N) { f(IC<I>{}); static_for<I + 1, N>(f); }
}
__device__ __forceinline__ float transpose_reduce(float (&x)[16], int c) {
#define TR_STEP(HALF, CTRL) _Pragma("unroll") for (int i = 0; i < (HALF); ++i) { \
    const float lo_ = x[i], hi_ = x[i + (HALF)]; const float t1_ = lo_ + dpp<CTRL>(lo_), t2_ = hi_ + dpp<CTRL>(hi_); \
    x[i] = (c & (HALF)) ? t2_ : t1_; }
  TR_STEP(8, ROR8) TR_STEP(4, XOR7) TR_STEP(2, XOR2) TR_STEP(1, XOR1)
#undef TR_STEP
  return x[0];
}

constexpr double TAU = 27.0;
constexpr int RING = 8;
constexpr int XP = 15, PRE = 24;
constexpr int D0 = 33, RLD = 40;
constexpr int PD0 = 145;
static_assert(D0 + 3 * 24 + 4 < 124, "wo lands before its use");
constexpr int slot_at(int n, int first, int cnt) { return (n >= first && (n - first) % 3 == 0 && (n - first) / 3 < cnt) ? (n - first) / 3 : -1; }
constexpr bool gated(int n) { return (n >= 2 && n <= 11) || (n > 11 && n <= XP ? (n & 1) != 0 : (n & 1) == 0); }
constexpr int gate_no(int n) { int g = 0; for (int m = 0; m < n; ++m) g += gated(m); return g; }
constexpr int EXTRA[18] = {0, 2, 2, 2, 2, 2, 10, 10, 2, -11, 4, 0, -9, 0, 0, 0, 0, 4};
constexpr int due_at(int n) { double t = 0; for (int g = 0; g < gate_no(n); ++g) t += 2 * TAU + (g < 18 ? EXTRA[g] : g == 41 ? 4 : 0); return (int)t; }
constexpr int target(int u) {
  const int drip = PRE + 2 * ((u + 8) / 8), mid = u >= 64 ? 64 + RING + 2 : 0;
  int t = (u + 1 + RING) & ~1;
  t = t < drip ? drip : t; t = t < mid ? mid : t;
  return u < 0 ? PRE : (t > 128 ? 128 : t);
}
constexpr int RCOL[12] = {33, 62, 8, 25, 32, 9, 36, 28, 40, 48, 41, 0}, OCOL[6] = {33, 21, 1, 16, 57, 18};
constexpr int ord(int n) { int r = 0; for (int k = 0; k < 6; ++k) if ((n >> k) & 1) r ^= OCOL[k]; return r; }
constexpr bool ord_is_bijective() { unsigned long long m = 0; for (int n = 0; n < 64; ++n) m |= 1ull << ord(n); return m == ~0ull; }
static_assert(ord_is_bijective(), "every piece is read once");
constexpr unsigned long long rtab(int k0) { unsigned long long t = 0; for (int i = 0; i < 8; ++i) { int r = 0; for (int k = 0; k < 3; ++k) if ((i >> k) & 1) r ^= RCOL[k0 + k]; t |= (unsigned long long)r << (8 * i); } return t; }
template <int DUE> __device__ __forceinline__ void gate(unsigned base) {
  asm volatile("1:\n\ts_memrealtime vcc\n\ts_waitcnt lgkmcnt(0)\n\ts_sub_u32 vcc_lo, vcc_lo, %0\n\ts_cmp_ge_i32 vcc_lo, %1\n\ts_cbranch_scc1 2f\n\ts_sleep 1\n\ts_branch 1b\n2:"
               :: "s"(base), "n"(DUE) : "vcc", "scc");
}

__global__ void __launch_bounds__(NTHR, 2)
fused_decode(const u16* x, const u16* __restrict__ kc, const u16* __restrict__ vc, const u16* __restrict__ wq,
             const u16* __restrict__ wo, const u16* __restrict__ wg, const u16* __restrict__ wu, const u16* __restrict__ wd,
             u16* __restrict__ out, unsigned char* ws) {
  __shared__ __attribute__((aligned(16))) float lds[L_END];
  SB();
  const int tid = threadIdx.x, lane = tid & 63, w = __builtin_amdgcn_readfirstlane(tid >> 6);
  const int bid = blockIdx.x, j = bid >> 3;
  const int h = (bid & 7) * 4 + (j >> 3), b = (j & 7) >> 1, s = j & 1;
  const int pidx = 2 * h + s;
  const int row = lane >> 4, c = lane & 15;
  const unsigned rid = (unsigned)(((b * NH + h) * 2 + s) * 8 + w);
  const unsigned rot = (unsigned)((rtab(0) >> (8 * (rid & 7))) ^ (rtab(3) >> (8 * ((rid >> 3) & 7))) ^ (rtab(6) >> (8 * ((rid >> 6) & 7))) ^ (rtab(9) >> (8 * (rid >> 9)))) & 63u;
  int voffs[4];
#pragma unroll
  for (int k = 0; k < 4; ++k) voffs[k] = lane * 16 + (int)((k ^ rot) & 3u) * 1024;
  const int rhi = (int)(rot >> 2) * 4096;
  u32x4 kv[128], wb[5], wst[25], rx4;
  unsigned tbase;
  {
    const unsigned pord = ((unsigned)bid & 0x3Eu) | (((unsigned)bid & 1u) << 6) | (((unsigned)bid & 64u) << 1) | ((unsigned)bid >> 7);
    const unsigned phi = (pord * (unsigned)(2 * TAU + .5)) >> 8;
    const int r1_ = rhi ^ ((ord(1) >> 2) * 4096);
    asm volatile("" :: "s"(phi), "s"(rhi), "s"(r1_), "v"(voffs[0]), "v"(voffs[ord(1) & 3]));
    SB();
    asm volatile("s_memrealtime vcc\n\ts_waitcnt lgkmcnt(0)\n\ts_add_u32 %0, vcc_lo, %1" : "=s"(tbase) : "s"(phi), "s"(kc), "s"(wd), "s"(ws), "s"(rhi), "s"(r1_), "v"(voffs[0]), "v"(voffs[ord(1) & 3]) : "vcc", "scc");
  }
  const rsrc_t rk = mk(kc + (size_t)rid * 32768, 65536);
  gate<0>(tbase);
  kv[0] = ld16<AUX_NT>(rk, voffs[0], rhi); kv[1] = ld16<AUX_NT>(rk, voffs[ord(1) & 3], rhi ^ ((ord(1) >> 2) * 4096));
  asm volatile("" : "+s"(x), "+s"(vc), "+s"(wq), "+s"(wo), "+s"(wg), "+s"(wu), "+s"(wd), "+s"(ws));
  SB();
  unsigned xr[5];
  {
    const rsrc_t rxw = mk(x + (w & 3) * DM, w < 4 ? DM * 2 : 0);
#pragma unroll
    for (int i = 0; i < 5; ++i) xr[i] = __builtin_amdgcn_raw_buffer_load_b32(rxw, lane * 4 + i * 256, 0, 0);
  }
  unsigned cw[3];
  {
    unsigned long long pc; unsigned len;
    asm volatile("s_getpc_b64 %0\n.Lfd_code0:\n\ts_mov_b32 %1, .Lfd_code1-.Lfd_code0" : "=s"(pc), "=s"(len));
    const rsrc_t rc = mk((const void*)pc, (j & 15) == 0 ? len : 0u);
#pragma unroll
    for (int i = 0; i < 3; ++i) cw[i] = __builtin_amdgcn_raw_buffer_load_b32(rc, w == 5 ? lane * 128 : OOB, i * 8192, 0);
  }
  const rsrc_t rws = mk(ws, WS_END);
  const unsigned epoch = __builtin_amdgcn_raw_buffer_load_b32(rws, WS_EPOCH, 0, AUX_SC1);
  MEMBAR();
  SB();
  const rsrc_t rv = mk(vc + (size_t)rid * 32768, 65536);
  const int fb = (j >> 3) * 8 + (bid & 7);
  const bool ffn = fb < FF / 64;
  const int pc = ffn ? fb : 0;
  const rsrc_t ro = mk(wo + (size_t)h * DH * DM, DH * DM * 2);
  const rsrc_t rd = mk(wd + (size_t)(2 * pc + s) * 32 * DM, ffn ? 32 * DM * 2 : 0);
  const int xoff = WS_X + ((h * NB + b) * 8 + w) * 1024;
  const bool xuse = ffn || w < 4;
  const int krow = 160 * b + 40 * (w & 3) + (lane >> 3);
  const int pitch = w < 4 ? NH * DH * 2 : FF * 2, col = w < 4 ? h * DH + s * 64 : pc * 64;
  const rsrc_t rw = mk((w < 4 ? wq : s ? wu : wg) + col, xuse ? (unsigned)(DM * pitch - col * 2) : 0u);
  const int vw = krow * pitch + (lane & 7) * 16;
  unsigned lw[3];
  auto issue = [&](auto I) {
    constexpr int n = decltype(I)::value;
    if constexpr (gated(n)) gate<due_at(n)>(tbase);
    constexpr int m = ord(n & 63);
    kv[n] = ld16<AUX_NT>(n < 64 ? rk : rv, voffs[m & 3], rhi ^ ((m >> 2) * 4096));
    SB();
    constexpr int id = slot_at(n, D0, 25);
    if constexpr (n >= 2 && n < 7) wb[n - 2] = ld16<AUX_NT>(rw, vw, (n - 2) * 8 * pitch);
    if constexpr (n == 7 || n == 8) lw[n - 7] = __builtin_amdgcn_raw_buffer_load_b32(ro, lane < 10 ? lane * 128 : OOB, (((j & 7) * 8 + w) * 2 + n - 7) * 1280, 0);
    if constexpr (n == 9) lw[2] = __builtin_amdgcn_raw_buffer_load_b32(rd, lane < 10 ? lane * 128 : OOB, (b * 8 + w) * 1280, 0);
    if constexpr (n == XP) {
      MEMBAR();
      rx4 = ld16<AUX_SC1>(rws, xuse ? lane * 16 : OOB, xoff);
    }
    if constexpr (id >= 0) wst[id] = ld16<0>(id < 5 ? rd : ro, tid * 16, (id < 5 ? id : id - 5) * 8192);
    SB();
  };
  static_for<2, 4>(issue);
  if (w < 4) {
    float xv[10], sum = 0.f;
#pragma unroll
    for (int i = 0; i < 5; ++i) { xv[2 * i] = lo_f(xr[i]); xv[2 * i + 1] = hi_f(xr[i]); sum += xv[2 * i] + xv[2 * i + 1]; }
    const float mean = wave_sum(sum) * (1.f / DM);
    float sq = 0.f;
#pragma unroll
    for (int i = 0; i < 10; ++i) { const float d = xv[i] - mean; sq = fmaf(d, d, sq); }
    const float var = bfr(wave_sum(sq) * (1.f / DM));
    const float mb = bfr(mean), rs = 1.f / bfr(sqrtf(bfr(var + 1e-5f)));
#pragma unroll
    for (int i = 0; i < 10; ++i) lds[L_XN + (2 * lane + 128 * (i >> 1) + (i & 1)) * 4 + w] = bfr(bfr(xv[i] - mb) * rs);
  }
  SB();
  static_for<4, 6>(issue);
  __syncthreads();
  asm volatile("" :: "v"(cw[0]), "v"(cw[1]), "v"(cw[2]));
  const unsigned tag0 = epoch + 1u, tag1 = (epoch + 1u) * 0x9E3779B1u + 0x7F4A7C15u;
  {
    Acc8 a[4];
#pragma unroll
    for (int bb = 0; bb < 4; ++bb) a[bb].zero();
    static_for<0, 5>([&](auto T) {
      constexpr int t = decltype(T)::value;
      const f32x4 xn4 = *(const f32x4*)(lds + L_XN + (krow + 8 * t) * 4);
#pragma unroll
      for (int bb = 0; bb < 4; ++bb) { fma8(a[bb], xn4[bb], wb[t]); a[bb].pin(); }
      SB();
      if constexpr (t >= 1 && t < 4) issue(IC<5 + t>{});
    });
    float v[8];
#pragma unroll
    for (int k = 0; k < 8; ++k) { v[k] = fold16(fold32(a[0][k], a[2][k]), fold32(a[1][k], a[3][k])); v[k] += dpp<ROR8>(v[k]); }
    const bool up = lane & 8;
    *(f32x4*)(lds + L_PP + w * 256 + row * 64 + (lane & 7) * 8 + (up ? 4 : 0)) = f32x4{up ? v[4] : v[0], up ? v[5] : v[1], up ? v[6] : v[2], up ? v[7] : v[3]};
  }
  SB();
  issue(IC<9>{});
  __syncthreads();
  if (w < 4) {
    f32x2 v = {0.f, 0.f};
#pragma unroll
    for (int i = 0; i < 4; ++i) v += *(const f32x2*)(lds + L_PP + ((tid >> 7) * 4 + i) * 256 + 2 * (tid & 127));
    const u32x4 r = {__float_as_uint(v[0]), tag0, __float_as_uint(v[1]), tag1};
    const int o = (((tid >> 5) & 3) * 8 + (tid >> 7) * 4 + b) * 1024 + s * 512 + (tid & 31) * 16;
    __builtin_amdgcn_raw_buffer_store_b128(r, rws, (ffn || w < 2) ? o : OOB, WS_X + h * NB * 8192, AUX_SC1);
  } else if (w == 7) {
    __builtin_amdgcn_raw_buffer_store_b64(u32x2{tbase, tag0}, rws, lane == 0 ? (b * 64 + pidx) * 8 : OOB, WS_T0, AUX_SC1);
  }
  SB();
  static_for<10, PRE - 2>(issue);
  asm volatile("" :: "v"(lw[0]), "v"(lw[1]), "v"(lw[2]));
  if (__builtin_expect(xuse && __builtin_amdgcn_ballot_w64(((rx4[1] ^ tag0) | (rx4[3] ^ tag1)) != 0u) != 0ull, 0)) {
    unsigned spin = 0;
    do {
      MEMBAR();
      rx4 = ld16<AUX_SC1>(rws, lane * 16, xoff);
      __builtin_amdgcn_s_waitcnt(0x0F70);
    } while (__builtin_amdgcn_ballot_w64(((rx4[1] ^ tag0) | (rx4[3] ^ tag1)) != 0u) != 0ull && ++spin < SPIN_CAP);
  }
  *(f32x2*)(lds + L_QP + w * 128 + 2 * lane) = f32x2{__uint_as_float(rx4[0]), __uint_as_float(rx4[2])};
  SB();
  static_for<PRE - 2, PRE>(issue);
  __syncthreads();
  u32x4 q;
  {
    f32x4 s0 = {0.f, 0.f, 0.f, 0.f}, s1 = {0.f, 0.f, 0.f, 0.f};
    float gu = 0.f;
#pragma unroll
    for (int kq = 0; kq < 4; ++kq) {
      s0 += *(const f32x4*)(lds + L_QP + kq * 128 + 8 * c); s1 += *(const f32x4*)(lds + L_QP + kq * 128 + 8 * c + 4);
      gu += lds[L_QP + (4 + kq) * 128 + (lane >> 5) * 64 + 32 * s + (lane & 31)];
    }
    q[0] = pk_bf16(s0[0], s0[1]); q[1] = pk_bf16(s0[2], s0[3]); q[2] = pk_bf16(s1[0], s1[1]); q[3] = pk_bf16(s1[2], s1[3]);
    gu = bfr(gu);
    auto sw = __builtin_amdgcn_permlane32_swap(__float_as_uint(gu), __float_as_uint(gu), false, false);
    const float hv = bfr(__uint_as_float(sw[0]) * __uint_as_float(sw[1]));
    if (lane < 32) ((u16*)(lds + L_HH + w * 16))[lane] = (u16)(__float_as_uint(hv) >> 16);
    WAVE_SYNC();
  }

  f32x4 y[5];
  u32x4 bfrag[4][5];
  auto mfma = [](u32x4 a, u32x4 bb, f32x4 cacc) { return __builtin_amdgcn_mfma_f32_16x16x32_bf16(__builtin_bit_cast(bf16x8, a), __builtin_bit_cast(bf16x8, bb), cacc, 0, 0, 0); };
  auto side = [&](auto U) {
    constexpr int u = decltype(U)::value;
    constexpr int iw = slot_at(u - 1, D0, 25), ir = slot_at(u - 2, D0, 25);
    if constexpr (iw >= 0) {
      const int qq = (iw % 5) * 512 + tid;
      *(u32x4*)(lds + L_ST + ((iw / 5) & 1) * 32 * STP + (qq / 80) * STP + (qq % 80) * 4) = wst[iw];
      if constexpr (iw % 5 == 4) __syncthreads();
      SB();
    }
    if constexpr (ir >= 0 && ir % 5 == 4) {
      const __attribute__((address_space(3))) char* tp = (const __attribute__((address_space(3))) char*)(lds + L_ST + ((ir / 5) & 1) * 32 * STP) + (8 * row + (c >> 2)) * (STP * 4) + w * 160 + (c & 3) * 8;
      u32x4 f[5];
#pragma unroll
      for (int t = 0; t < 5; ++t) {
        const u32x2 lo = __builtin_bit_cast(u32x2, __builtin_amdgcn_ds_read_tr16_b64_v4i16((__attribute__((address_space(3))) s16x4*)(tp + t * 32)));
        const u32x2 hi = __builtin_bit_cast(u32x2, __builtin_amdgcn_ds_read_tr16_b64_v4i16((__attribute__((address_space(3))) s16x4*)(tp + 4 * STP * 4 + t * 32)));
        f[t] = u32x4{lo[0], lo[1], hi[0], hi[1]};
      }
      if constexpr (ir == 4) {
        u32x4 ha = *(const u32x4*)(lds + L_HH + w * 16 + row * 4);
        if (lane & 7) ha = u32x4{0u, 0u, 0u, 0u};
#pragma unroll
        for (int t = 0; t < 5; ++t) { y[t] = mfma(ha, f[t], f32x4{0.f, 0.f, 0.f, 0.f}); asm volatile("" : "+v"(y[t])); }
      } else {
#pragma unroll
        for (int t = 0; t < 5; ++t) { bfrag[ir / 5 - 1][t] = f[t]; asm volatile("" : "+v"(bfrag[ir / 5 - 1][t])); }
      }
      SB();
    }
  };

  constexpr float SCALE = 0.08838834764831845f, LOG2E = 1.4426950408889634f;
  float sc[4];
  {
    float xx[16];
    static_for<0, 64>([&](auto I) {
      constexpr int st = decltype(I)::value;
      float d = dot2(kv[st][0], q[0], 0.f);
      d = dot2(kv[st][1], q[1], d); d = dot2(kv[st][2], q[2], d); d = dot2(kv[st][3], q[3], d);
      asm volatile("" : "+v"(d));
      xx[st & 15] = d;
      SB();
      static_for<target(st - 1), target(st)>(issue);
      side(I);
      if constexpr ((st & 15) == 15) { sc[st >> 4] = bfr(bfr(transpose_reduce(xx, c)) * SCALE); SB(); }
    });
  }
  float mw = fmaxf(wave_max(fmaxf(fmaxf(sc[0], sc[1]), fmaxf(sc[2], sc[3]))), -3.38e38f);
  asm volatile("" : "+v"(mw));
  SB();
  static_for<target(63), target(64)>(issue);
  float p[4];
#pragma unroll
  for (int g = 0; g < 4; ++g) p[g] = __builtin_amdgcn_exp2f((sc[g] - mw) * LOG2E);
  const float lsum = wave_sum((p[0] + p[1]) + (p[2] + p[3]));
  const int rec = WS_REC + (b * NH + h) * 256;
  {
    const u32x4 r = {__float_as_uint(mw), tag0, __float_as_uint(lsum), tag1};
    __builtin_amdgcn_raw_buffer_store_b128(r, rws, lane == 0 ? (s * 8 + w) * 16 : OOB, rec, AUX_SC1);
  }
  SB();

  Acc8 acc; acc.zero();
  const bool red = w == 7 && bid < 160;
  const int rb = bid & 3, cg = bid >> 2;
  u32x4 r1;
  u32x2 rt;
  unsigned xo = 0;
  int tlag = 0;
  const bool odd = lane & 1;
  int so_ = (odd && row) ? OOB : (5 * w + (odd ? 4 : row)) * 8192 + (c >> 1) * 16;
  asm volatile("" : "+v"(so_));
  float fw = 0.f;
  static_for<0, 64>([&](auto I) {
    constexpr int st = decltype(I)::value;
    fma8(acc, dpp<BCAST + (st & 15)>(p[st >> 4]), kv[64 + st]);
    acc.pin();
    SB();
    if constexpr (st > 0) static_for<target(63 + st), target(64 + st)>(issue);
    side(IC<64 + st>{});
    if constexpr (st == RLD) { MEMBAR(); r1 = ld16<AUX_SC1>(rws, c * 16, rec); SB(); }
    if constexpr (st == RLD + 2) { xo = __builtin_amdgcn_raw_buffer_load_b16(mk(x + rb * DM, red ? DM * 2 : 0), (cg * 16 + 2 * (lane & 7) + (lane >> 5)) * 2, 0, 0); SB(); }
    if constexpr (st == RLD - 2) { MEMBAR(); rt = __builtin_amdgcn_raw_buffer_load_b64(rws, red ? lane * 8 : OOB, WS_T0 + rb * 512, AUX_SC1); SB(); }
    if constexpr (st == RLD + RING) {
      const int dt = (int)(rt[0] - tbase);
      tlag = (int)wave_max(rt[1] == tag0 && dt < 150 ? (float)dt : -1000.f);
      asm volatile("" : "+v"(tlag));
      SB();
    }
    if constexpr (st == RLD + RING + 2) {
      if (__builtin_expect(__builtin_amdgcn_ballot_w64(((r1[1] ^ tag0) | (r1[3] ^ tag1)) != 0u) != 0ull, 0)) {
        unsigned spin = 0;
        do {
          __builtin_amdgcn_s_sleep(8); MEMBAR();
          r1 = ld16<AUX_SC1>(rws, c * 16, rec);
          __builtin_amdgcn_s_waitcnt(0x0F70);
        } while (__builtin_amdgcn_ballot_w64(((r1[1] ^ tag0) | (r1[3] ^ tag1)) != 0u) != 0ull && ++spin < SPIN_CAP);
      }
      const float mi = __uint_as_float(r1[0]), li = __uint_as_float(r1[2]);
      const float M = row_max(mi);
      const float L = row_sum(li * __builtin_amdgcn_exp2f((mi - M) * LOG2E));
      fw = __builtin_amdgcn_exp2f((mw - M) * LOG2E) / L;
      asm volatile("" : "+v"(fw));
      SB();
    }
  });
  {
    const float e0 = fold32(acc[0], acc[4]), e1 = fold32(acc[1], acc[5]), e2 = fold32(acc[2], acc[6]), e3 = fold32(acc[3], acc[7]);
    ((unsigned*)lds)[L_OP + w * 72 + 4 * c + row] = pk_bf16(fold16(e0, e2) * fw, fold16(e1, e3) * fw);
  }
  __syncthreads();
  {
    u32x4 af[4];
#pragma unroll
    for (int ks = 0; ks < 4; ++ks) af[ks] = *(const u32x4*)(lds + L_OP + (lane & 7) * 72 + ks * 16 + row * 4);
#pragma unroll
    for (int ks = 0; ks < 4; ++ks)
#pragma unroll
      for (int t = 0; t < 5; ++t) y[t] = mfma(af[ks], bfrag[ks][t], y[t]);
    float sm[5];
#pragma unroll
    for (int t = 0; t < 5; ++t) sm[t] = (y[t][0] + y[t][1]) + (y[t][2] + y[t][3]);
    const float v01 = fold16(sm[0], sm[1]), v23 = fold16(sm[2], sm[3]), v4 = fold16(sm[4], sm[4]);
    const float v = lane < 32 ? v01 : v23;
    const float vn = dpp<XOR1>(v), v4n = dpp<XOR1>(v4);
    __builtin_amdgcn_raw_buffer_store_b128(u32x4{__float_as_uint(odd ? v4n : v), tag0, __float_as_uint(odd ? v4 : vn), tag1}, rws,
                                           so_, WS_SLAB + b * 64 * DM * 8 + pidx * 128, AUX_SC1);
  }

  if (red) {
    const int so = WS_SLAB + rb * 64 * DM * 8 + cg * 8192, vo = lane * 16;
    auto poll = [&](u32x4 (&t)[8]) {
      MEMBAR();
#pragma unroll
      for (int i = 0; i < 8; ++i) { t[i] = ld16<AUX_SC1>(rws, vo + (i & 3) * 1024, so + (i >> 2) * 4096); SB(); }
    };
    f32x2 f;
    auto take = [&](u32x4 (&t)[8]) -> bool {
      unsigned bad = 0;
      f = f32x2{0.f, 0.f};
#pragma unroll
      for (int i = 0; i < 8; ++i) {
        bad |= (t[i][1] ^ tag0) | (t[i][3] ^ tag1); f[0] += __uint_as_float(t[i][0]); f[1] += __uint_as_float(t[i][2]);
        asm volatile("" : "+v"(bad), "+v"(f));
      }
      return __builtin_amdgcn_ballot_w64(bad != 0u) == 0ull;
    };
    u32x4 ta[8];
    gate<due_at(126) + PD0>(tbase + (unsigned)__builtin_amdgcn_readfirstlane(tlag));
    for (unsigned spin = 0; spin < SPIN_CAP; ++spin) { poll(ta); if (take(ta)) break; }
    float v = fold32(f[0], f[1]);
    v = fold16(v, v); v += dpp<ROR8>(v);
    __builtin_amdgcn_raw_buffer_store_b16(__builtin_bit_cast(u16, (__bf16)(lo_f(xo) + v)), mk(out, NB * DM * 2),
                                          (lane & 24) == 0 ? (rb * DM + cg * 16 + 2 * (lane & 7) + (lane >> 5)) * 2 : OOB, 0, 0);
    if (lane == 0 && bid == 0) __builtin_amdgcn_raw_buffer_store_b32(tag0, rws, WS_EPOCH, 0, AUX_SC1);
  }
  asm volatile(".Lfd_code1:");
}

}

extern "C" void kernel_launch(void* const* d_in, const int* in_sizes, int n_in, void* d_out, int out_size, void* d_ws, size_t ws_size,
                              hipStream_t stream) {
  (void)in_sizes; (void)n_in; (void)out_size; (void)ws_size;
  fused_decode<<<NWG, NTHR, 0, stream>>>((const u16*)d_in[0], (const u16*)d_in[1], (const u16*)d_in[2], (const u16*)d_in[3],
                                         (const u16*)d_in[4], (const u16*)d_in[6], (const u16*)d_in[7], (const u16*)d_in[8],
                                         (u16*)d_out, (unsigned char*)d_ws);
}
```

```cpp
#include <hip/hip_runtime.h>
#include <hip/hip_bf16.h>
#include <stdint.h>

namespace {

constexpr int NB = 4, DM = 640, NH = 32, DH = 128, FF = 1728;
constexpr int NWG = 256, NTHR = 512;

typedef unsigned short u16;
typedef unsigned u32x4 __attribute__((ext_vector_type(4)));
typedef unsigned u32x2 __attribute__((ext_vector_type(2)));
typedef float f32x4 __attribute__((ext_vector_type(4)));
typedef float f32x2 __attribute__((ext_vector_type(2)));
typedef __bf16 bf16x2 __attribute__((ext_vector_type(2)));
typedef short s16x4 __attribute__((ext_vector_type(4)));
typedef __bf16 bf16x8 __attribute__((ext_vector_type(8)));
typedef __amdgpu_buffer_rsrc_t rsrc_t;

constexpr int AUX_NT = 2, AUX_SC1 = 16, RSRC_W3 = 0x00020000;

constexpr int WS_EPOCH = 0, WS_T0 = 2048, WS_REC = 4096, WS_SLAB = WS_REC + NB * NH * 16 * 16, WS_X = WS_SLAB + NB * 64 * DM * 8, WS_END = WS_X + NH * NB * 8192;
constexpr unsigned SPIN_CAP = 1u << 18;
constexpr int OOB = 1 << 28;

constexpr int L_XN = 0;
constexpr int L_PP = L_XN + 640 * 4;
constexpr int L_QP = L_PP + 8 * 256;
constexpr int L_HH = L_QP + 8 * 128;
constexpr int L_OP = L_HH + 8 * 16;
constexpr int L_ST = L_OP + 8 * 72;
constexpr int STP = 328;
constexpr int L_END = L_ST + 2 * 32 * STP;

__device__ __forceinline__ rsrc_t mk(const void* p, unsigned bytes) {
  return __builtin_amdgcn_make_buffer_rsrc(const_cast<void*>(p), 0, (int)bytes, RSRC_W3);
}
template <int AUX> __device__ __forceinline__ u32x4 ld16(rsrc_t r, int voff, int soff) {
  return __builtin_amdgcn_raw_buffer_load_b128(r, voff, soff, AUX);
}
__device__ __forceinline__ float lo_f(unsigned u) { return __builtin_bit_cast(float, u << 16); }
__device__ __forceinline__ float hi_f(unsigned u) { return __builtin_bit_cast(float, u & 0xffff0000u); }
__device__ __forceinline__ float bfr(float f) { return (float)(__bf16)f; }
__device__ __forceinline__ unsigned pk_bf16(float lo, float hi) {
  bf16x2 v = {(__bf16)lo, (__bf16)hi};
  return __builtin_bit_cast(unsigned, v);
}
__device__ __forceinline__ float dot2(unsigned a, unsigned b, float acc) {
  return __builtin_amdgcn_fdot2_f32_bf16(__builtin_bit_cast(bf16x2, a), __builtin_bit_cast(bf16x2, b), acc, false);
}
template <int CTRL> __device__ __forceinline__ float dpp(float x) {
  return __builtin_bit_cast(float, __builtin_amdgcn_mov_dpp(__builtin_bit_cast(int, x), CTRL, 0xf, 0xf, true));
}
constexpr int XOR1 = 0xB1, XOR2 = 0x4E, XOR7 = 0x141, ROR8 = 0x128, BCAST = 0x150;
__device__ __forceinline__ float fold32(float a, float b) {
  auto s = __builtin_amdgcn_permlane32_swap(__builtin_bit_cast(unsigned, a), __builtin_bit_cast(unsigned, b), false, false);
  return __builtin_bit_cast(float, (unsigned)s[0]) + __builtin_bit_cast(float, (unsigned)s[1]);
}
__device__ __forceinline__ float fold16(float a, float b) {
  auto s = __builtin_amdgcn_permlane16_swap(__builtin_bit_cast(unsigned, a), __builtin_bit_cast(unsigned, b), false, false);
  return __builtin_bit_cast(float, (unsigned)s[0]) + __builtin_bit_cast(float, (unsigned)s[1]);
}
__device__ __forceinline__ float row_sum(float v) {
  v += dpp<XOR1>(v); v += dpp<XOR2>(v); v += dpp<XOR7>(v); v += dpp<ROR8>(v); return v;
}
__device__ __forceinline__ float row_max(float v) {
  v = fmaxf(v, dpp<XOR1>(v)); v = fmaxf(v, dpp<XOR2>(v)); v = fmaxf(v, dpp<XOR7>(v)); v = fmaxf(v, dpp<ROR8>(v)); return v;
}
__device__ __forceinline__ float wave_sum(float v) { v = row_sum(v); v = fold16(v, v); return fold32(v, v); }
__device__ __forceinline__ float wave_max(float v) {
  v = row_max(v);
  auto s = __builtin_amdgcn_permlane16_swap(__builtin_bit_cast(unsigned, v), __builtin_bit_cast(unsigned, v), false, false);
  v = fmaxf(__builtin_bit_cast(float, (unsigned)s[0]), __builtin_bit_cast(float, (unsigned)s[1]));
  auto t = __builtin_amdgcn_permlane32_swap(__builtin_bit_cast(unsigned, v), __builtin_bit_cast(unsigned, v), false, false);
  return fmaxf(__builtin_bit_cast(float, (unsigned)t[0]), __builtin_bit_cast(float, (unsigned)t[1]));
}
struct Acc8 {
  f32x2 v[4];
  __device__ __forceinline__ void zero() { v[0] = v[1] = v[2] = v[3] = f32x2{0.f, 0.f}; }
  __device__ __forceinline__ float operator[](int i) const { return v[i >> 1][i & 1]; }
  __device__ __forceinline__ void pin() { asm volatile("" : "+v"(v[0]), "+v"(v[1]), "+v"(v[2]), "+v"(v[3])); }
};
__device__ __forceinline__ void fma8(Acc8& acc, float s, u32x4 v) {
  const f32x2 ss = {s, s};
#pragma unroll
  for (int k = 0; k < 4; ++k) acc.v[k] = __builtin_elementwise_fma(ss, f32x2{lo_f(v[k]), hi_f(v[k])}, acc.v[k]);
}
#define MEMBAR() asm volatile("" ::: "memory")
#define SB() __builtin_amdgcn_sched_barrier(0)
#define WAVE_SYNC() do { __builtin_amdgcn_fence(__ATOMIC_RELEASE, "wavefront"); __builtin_amdgcn_wave_barrier(); __builtin_amdgcn_fence(__ATOMIC_ACQUIRE, "wavefront"); } while (0)

template <int I> struct IC { static constexpr int value = I; };
template <int I, int N, class F> __device__ __forceinline__ void static_for(F&& f) {
  if constexpr (I < N) { f(IC<I>{}); static_for<I + 1, N>(f); }
}
__device__ __forceinline__ float transpose_reduce(float (&x)[16], int c) {
#define TR_STEP(HALF, CTRL) _Pragma("unroll") for (int i = 0; i < (HALF); ++i) { \
    const float lo_ = x[i], hi_ = x[i + (HALF)]; const float t1_ = lo_ + dpp<CTRL>(lo_), t2_ = hi_ + dpp<CTRL>(hi_); \
    x[i] = (c & (HALF)) ? t2_ : t1_; }
  TR_STEP(8, ROR8) TR_STEP(4, XOR7) TR_STEP(2, XOR2) TR_STEP(1, XOR1)
#undef TR_STEP
  return x[0];
}

constexpr double TAU = 27.0;
constexpr int RING = 8;
constexpr int XP = 15, PRE = 24;
constexpr int D0 = 33, RLD = 40;
constexpr int PD0 = 145;
static_assert(D0 + 3 * 24 + 4 < 124, "wo lands before its use");
constexpr int slot_at(int n, int first, int cnt) { return (n >= first && (n - first) % 3 == 0 && (n - first) / 3 < cnt) ? (n - first) / 3 : -1; }
constexpr bool gated(int n) { return (n >= 2 && n <= 11) || (n > 11 && n <= XP ? (n & 1) != 0 : (n & 1) == 0); }
constexpr int gate_no(int n) { int g = 0; for (int m = 0; m < n; ++m) g += gated(m); return g; }
constexpr int EXTRA[18] = {0, 2, 2, 2, 2, 2, 10, 10, 2, -11, 4, 0, -9, 0, 0, 0, 0, 4};
constexpr int due_at(int n) { double t = 0; for (int g = 0; g < gate_no(n); ++g) t += 2 * TAU + (g < 18 ? EXTRA[g] : g == 41 ? 4 : 0); return (int)t; }
constexpr int target(int u) {
  const int drip = PRE + 2 * ((u + 8) / 8), mid = u >= 64 ? 64 + RING + 2 : 0;
  int t = (u + 1 + RING) & ~1;
  t = t < drip ? drip : t; t = t < mid ? mid : t;
  return u < 0 ? PRE : (t > 128 ? 128 : t);
}
constexpr int RCOL[12] = {33, 62, 8, 25, 32, 9, 36, 28, 40, 48, 41, 0}, OCOL[6] = {33, 21, 1, 16, 57, 18};
constexpr int ord(int n) { int r = 0; for (int k = 0; k < 6; ++k) if ((n >> k) & 1) r ^= OCOL[k]; return r; }
constexpr bool ord_is_bijective() { unsigned long long m = 0; for (int n = 0; n < 64; ++n) m |= 1ull << ord(n); return m == ~0ull; }
static_assert(ord_is_bijective(), "every piece is read once");
constexpr unsigned long long rtab(int k0) { unsigned long long t = 0; for (int i = 0; i < 8; ++i) { int r = 0; for (int k = 0; k < 3; ++k) if ((i >> k) & 1) r ^= RCOL[k0 + k]; t |= (unsigned long long)r << (8 * i); } return t; }
template <int DUE> __device__ __forceinline__ void gate(unsigned base) {
  asm volatile("1:\n\ts_memrealtime vcc\n\ts_waitcnt lgkmcnt(0)\n\ts_sub_u32 vcc_lo, vcc_lo, %0\n\ts_cmp_ge_i32 vcc_lo, %1\n\ts_cbranch_scc1 2f\n\ts_sleep 1\n\ts_branch 1b\n2:"
               :: "s"(base), "n"(DUE) : "vcc", "scc");
}

__global__ void __launch_bounds__(NTHR, 2)
fused_decode(const u16* x, const u16* __restrict__ kc, const u16* __restrict__ vc, const u16* __restrict__ wq,
             const u16* __restrict__ wo, const u16* __restrict__ wg, const u16* __restrict__ wu, const u16* __restrict__ wd,
             u16* __restrict__ out, unsigned char* ws) {
  __shared__ __attribute__((aligned(16))) float lds[L_END];
  SB();
  const int tid = threadIdx.x, lane = tid & 63, w = __builtin_amdgcn_readfirstlane(tid >> 6);
  const int bid = blockIdx.x, j = bid >> 3;
  const int h = (bid & 7) * 4 + (j >> 3), b = (j & 7) >> 1, s = j & 1;
  const int pidx = 2 * h + s;
  const int row = lane >> 4, c = lane & 15;
  const unsigned rid = (unsigned)(((b * NH + h) * 2 + s) * 8 + w);
  const unsigned rot = (unsigned)((rtab(0) >> (8 * (rid & 7))) ^ (rtab(3) >> (8 * ((rid >> 3) & 7))) ^ (rtab(6) >> (8 * ((rid >> 6) & 7))) ^ (rtab(9) >> (8 * (rid >> 9)))) & 63u;
  int voffs[4];
#pragma unroll
  for (int k = 0; k < 4; ++k) voffs[k] = lane * 16 + (int)((k ^ rot) & 3u) * 1024;
  const int rhi = (int)(rot >> 2) * 4096;
  u32x4 kv[128], wb[5], wst[25], rx4;
  unsigned tbase;
  {
    const unsigned pord = ((unsigned)bid & 0x3Eu) | (((unsigned)bid & 1u) << 6) | (((unsigned)bid & 64u) << 1) | ((unsigned)bid >> 7);
    const unsigned phi = (pord * (unsigned)(2 * TAU + .5)) >> 8;
    const int r1_ = rhi ^ ((ord(1) >> 2) * 4096);
    asm volatile("" :: "s"(phi), "s"(rhi), "s"(r1_), "v"(voffs[0]), "v"(voffs[ord(1) & 3]));
    SB();
    asm volatile("s_memrealtime vcc\n\ts_waitcnt lgkmcnt(0)\n\ts_add_u32 %0, vcc_lo, %1" : "=s"(tbase) : "s"(phi), "s"(kc), "s"(wd), "s"(ws), "s"(rhi), "s"(r1_), "v"(voffs[0]), "v"(voffs[ord(1) & 3]) : "vcc", "scc");
  }
  const rsrc_t rk = mk(kc + (size_t)rid * 32768, 65536);
  gate<0>(tbase);
  kv[0] = ld16<AUX_NT>(rk, voffs[0], rhi); kv[1] = ld16<AUX_NT>(rk, voffs[ord(1) & 3], rhi ^ ((ord(1) >> 2) * 4096));
  asm volatile("" : "+s"(x), "+s"(vc), "+s"(wq), "+s"(wo), "+s"(wg), "+s"(wu), "+s"(wd), "+s"(ws));
  SB();
  unsigned xr[5];
  {
    const rsrc_t rxw = mk(x + (w & 3) * DM, w < 4 ? DM * 2 : 0);
#pragma unroll
    for (int i = 0; i < 5; ++i) xr[i] = __builtin_amdgcn_raw_buffer_load_b32(rxw, lane * 4 + i * 256, 0, 0);
  }
  unsigned cw[3];
  {
    unsigned long long pc; unsigned len;
    asm volatile("s_getpc_b64 %0\n.Lfd_code0:\n\ts_mov_b32 %1, .Lfd_code1-.Lfd_code0" : "=s"(pc), "=s"(len));
    const rsrc_t rc = mk((const void*)pc, (j & 15) == 0 ? len : 0u);
#pragma unroll
    for (int i = 0; i < 3; ++i) cw[i] = __builtin_amdgcn_raw_buffer_load_b32(rc, w == 5 ? lane * 128 : OOB, i * 8192, 0);
  }
  const rsrc_t rws = mk(ws, WS_END);
  const unsigned epoch = __builtin_amdgcn_raw_buffer_load_b32(rws, WS_EPOCH, 0, AUX_SC1);
  MEMBAR();
  SB();
  const rsrc_t rv = mk(vc + (size_t)rid * 32768, 65536);
  const int fb = (j >> 3) * 8 + (bid & 7);
  const bool ffn = fb < FF / 64;
  const int pc = ffn ? fb : 0;
  const rsrc_t ro = mk(wo + (size_t)h * DH * DM, DH * DM * 2);
  const rsrc_t rd = mk(wd + (size_t)(2 * pc + s) * 32 * DM, ffn ? 32 * DM * 2 : 0);
  const int xoff = WS_X + ((h * NB + b) * 8 + w) * 1024;
  const bool xuse = ffn || w < 4;
  const int krow = 160 * b + 40 * (w & 3) + (lane >> 3);
  const int pitch = w < 4 ? NH * DH * 2 : FF * 2, col = w < 4 ? h * DH + s * 64 : pc * 64;
  const rsrc_t rw = mk((w < 4 ? wq : s ? wu : wg) + col, xuse ? (unsigned)(DM * pitch - col * 2) : 0u);
  const int vw = krow * pitch + (lane & 7) * 16;
  unsigned lw[3];
  auto issue = [&](auto I) {
    constexpr int n = decltype(I)::value;
    if constexpr (gated(n)) gate<due_at(n)>(tbase);
    constexpr int m = ord(n & 63);
    kv[n] = ld16<AUX_NT>(n < 64 ? rk : rv, voffs[m & 3], rhi ^ ((m >> 2) * 4096));
    SB();
    constexpr int id = slot_at(n, D0, 25);
    if constexpr (n >= 2 && n < 7) wb[n - 2] = ld16<AUX_NT>(rw, vw, (n - 2) * 8 * pitch);
    if constexpr (n == 7 || n == 8) lw[n - 7] = __builtin_amdgcn_raw_buffer_load_b32(ro, lane < 10 ? lane * 128 : OOB, (((j & 7) * 8 + w) * 2 + n - 7) * 1280, 0);
    if constexpr (n == 9) lw[2] = __builtin_amdgcn_raw_buffer_load_b32(rd, lane < 10 ? lane * 128 : OOB, (b * 8 + w) * 1280, 0);
    if constexpr (n == XP) {
      MEMBAR();
      rx4 = ld16<AUX_SC1>(rws, xuse ? lane * 16 : OOB, xoff);
    }
    if constexpr (id >= 0) wst[id] = ld16<0>(id < 5 ? rd : ro, tid * 16, (id < 5 ? id : id - 5) * 8192);
    SB();
  };
  static_for<2, 4>(issue);
  if (w < 4) {
    float xv[10], sum = 0.f;
#pragma unroll
    for (int i = 0; i < 5; ++i) { xv[2 * i] = lo_f(xr[i]); xv[2 * i + 1] = hi_f(xr[i]); sum += xv[2 * i] + xv[2 * i + 1]; }
    const float mean = wave_sum(sum) * (1.f / DM);
    float sq = 0.f;
#pragma unroll
    for (int i = 0; i < 10; ++i) { const float d = xv[i] - mean; sq = fmaf(d, d, sq); }
    const float var = bfr(wave_sum(sq) * (1.f / DM));
    const float mb = bfr(mean), rs = 1.f / bfr(sqrtf(bfr(var + 1e-5f)));
#pragma unroll
    for (int i = 0; i < 10; ++i) lds[L_XN + (2 * lane + 128 * (i >> 1) + (i & 1)) * 4 + w] = bfr(bfr(xv[i] - mb) * rs);
  }
  SB();
  static_for<4, 6>(issue);
  __syncthreads();
  asm volatile("" :: "v"(cw[0]), "v"(cw[1]), "v"(cw[2]));
  const unsigned tag0 = epoch + 1u, tag1 = (epoch + 1u) * 0x9E3779B1u + 0x7F4A7C15u;
  {
    Acc8 a[4];
#pragma unroll
    for (int bb = 0; bb < 4; ++bb) a[bb].zero();
    static_for<0, 5>([&](auto T) {
      constexpr int t = decltype(T)::value;
      const f32x4 xn4 = *(const f32x4*)(lds + L_XN + (krow + 8 * t) * 4);
#pragma unroll
      for (int bb = 0; bb < 4; ++bb) { fma8(a[bb], xn4[bb], wb[t]); a[bb].pin(); }
      SB();
      if constexpr (t >= 1 && t < 4) issue(IC<5 + t>{});
    });
    float v[8];
#pragma unroll
    for (int k = 0; k < 8; ++k) { v[k] = fold16(fold32(a[0][k], a[2][k]), fold32(a[1][k], a[3][k])); v[k] += dpp<ROR8>(v[k]); }
    const bool up = lane & 8;
    *(f32x4*)(lds + L_PP + w * 256 + row * 64 + (lane & 7) * 8 + (up ? 4 : 0)) = f32x4{up ? v[4] : v[0], up ? v[5] : v[1], up ? v[6] : v[2], up ? v[7] : v[3]};
  }
  SB();
  issue(IC<9>{});
  __syncthreads();
  if (w < 4) {
    f32x2 v = {0.f, 0.f};
#pragma unroll
    for (int i = 0; i < 4; ++i) v += *(const f32x2*)(lds + L_PP + ((tid >> 7) * 4 + i) * 256 + 2 * (tid & 127));
    const u32x4 r = {__float_as_uint(v[0]), tag0, __float_as_uint(v[1]), tag1};
    const int o = (((tid >> 5) & 3) * 8 + (tid >> 7) * 4 + b) * 1024 + s * 512 + (tid & 31) * 16;
    __builtin_amdgcn_raw_buffer_store_b128(r, rws, (ffn || w < 2) ? o : OOB, WS_X + h * NB * 8192, AUX_SC1);
  } else if (w == 7) {
    __builtin_amdgcn_raw_buffer_store_b64(u32x2{tbase, tag0}, rws, lane == 0 ? (b * 64 + pidx) * 8 : OOB, WS_T0, AUX_SC1);
  }
  SB();
  static_for<10, PRE - 2>(issue);
  asm volatile("" :: "v"(lw[0]), "v"(lw[1]), "v"(lw[2]));
  if (__builtin_expect(xuse && __builtin_amdgcn_ballot_w64(((rx4[1] ^ tag0) | (rx4[3] ^ tag1)) != 0u) != 0ull, 0)) {
    unsigned spin = 0;
    do {
      MEMBAR();
      rx4 = ld16<AUX_SC1>(rws, lane * 16, xoff);
      __builtin_amdgcn_s_waitcnt(0x0F70);
    } while (__builtin_amdgcn_ballot_w64(((rx4[1] ^ tag0) | (rx4[3] ^ tag1)) != 0u) != 0ull && ++spin < SPIN_CAP);
  }
  *(f32x2*)(lds + L_QP + w * 128 + 2 * lane) = f32x2{__uint_as_float(rx4[0]), __uint_as_float(rx4[2])};
  SB();
  static_for<PRE - 2, PRE>(issue);
  __syncthreads();
  u32x4 q;
  {
    f32x4 s0 = {0.f, 0.f, 0.f, 0.f}, s1 = {0.f, 0.f, 0.f, 0.f};
    float gu = 0.f;
#pragma unroll
    for (int kq = 0; kq < 4; ++kq) {
      s0 += *(const f32x4*)(lds + L_QP + kq * 128 + 8 * c); s1 += *(const f32x4*)(lds + L_QP + kq * 128 + 8 * c + 4);
      gu += lds[L_QP + (4 + kq) * 128 + (lane >> 5) * 64 + 32 * s + (lane & 31)];
    }
    q[0] = pk_bf16(s0[0], s0[1]); q[1] = pk_bf16(s0[2], s0[3]); q[2] = pk_bf16(s1[0], s1[1]); q[3] = pk_bf16(s1[2], s1[3]);
    gu = bfr(gu);
    auto sw = __builtin_amdgcn_permlane32_swap(__float_as_uint(gu), __float_as_uint(gu), false, false);
    const float hv = bfr(__uint_as_float(sw[0]) * __uint_as_float(sw[1]));
    if (lane < 32) ((u16*)(lds + L_HH + w * 16))[lane] = (u16)(__float_as_uint(hv) >> 16);
    WAVE_SYNC();
  }

  f32x4 y[5];
  u32x4 bfrag[4][5];
  auto mfma = [](u32x4 a, u32x4 bb, f32x4 cacc) { return __builtin_amdgcn_mfma_f32_16x16x32_bf16(__builtin_bit_cast(bf16x8, a), __builtin_bit_cast(bf16x8, bb), cacc, 0, 0, 0); };
  auto side = [&](auto U) {
    constexpr int u = decltype(U)::value;
    constexpr int iw = slot_at(u - 1, D0, 25), ir = slot_at(u - 2, D0, 25);
    if constexpr (iw >= 0) {
      const int qq = (iw % 5) * 512 + tid;
      *(u32x4*)(lds + L_ST + ((iw / 5) & 1) * 32 * STP + (qq / 80) * STP + (qq % 80) * 4) = wst[iw];
      if constexpr (iw % 5 == 4) __syncthreads();
      SB();
    }
    if constexpr (ir >= 0 && ir % 5 == 4) {
      const __attribute__((address_space(3))) char* tp = (const __attribute__((address_space(3))) char*)(lds + L_ST + ((ir / 5) & 1) * 32 * STP) + (8 * row + (c >> 2)) * (STP * 4) + w * 160 + (c & 3) * 8;
      u32x4 f[5];
#pragma unroll
      for (int t = 0; t < 5; ++t) {
        const u32x2 lo = __builtin_bit_cast(u32x2, __builtin_amdgcn_ds_read_tr16_b64_v4i16((__attribute__((address_space(3))) s16x4*)(tp + t * 32)));
        const u32x2 hi = __builtin_bit_cast(u32x2, __builtin_amdgcn_ds_read_tr16_b64_v4i16((__attribute__((address_space(3))) s16x4*)(tp + 4 * STP * 4 + t * 32)));
        f[t] = u32x4{lo[0], lo[1], hi[0], hi[1]};
      }
      if constexpr (ir == 4) {
        u32x4 ha = *(const u32x4*)(lds + L_HH + w * 16 + row * 4);
        if (lane & 7) ha = u32x4{0u, 0u, 0u, 0u};
#pragma unroll
        for (int t = 0; t < 5; ++t) { y[t] = mfma(ha, f[t], f32x4{0.f, 0.f, 0.f, 0.f}); asm volatile("" : "+v"(y[t])); }
      } else {
#pragma unroll
        for (int t = 0; t < 5; ++t) { bfrag[ir / 5 - 1][t] = f[t]; asm volatile("" : "+v"(bfrag[ir / 5 - 1][t])); }
      }
      SB();
    }
  };

  constexpr float SCALE = 0.08838834764831845f, LOG2E = 1.4426950408889634f;
  float sc[4];
  {
    float xx[16];
    static_for<0, 64>([&](auto I) {
      constexpr int st = decltype(I)::value;
      float d = dot2(kv[st][0], q[0], 0.f);
      d = dot2(kv[st][1], q[1], d); d = dot2(kv[st][2], q[2], d); d = dot2(kv[st][3], q[3], d);
      asm volatile("" : "+v"(d));
      xx[st & 15] = d;
      SB();
      static_for<target(st - 1), target(st)>(issue);
      side(I);
      if constexpr ((st & 15) == 15) { sc[st >> 4] = bfr(bfr(transpose_reduce(xx, c)) * SCALE); SB(); }
    });
  }
  float mw = fmaxf(wave_max(fmaxf(fmaxf(sc[0], sc[1]), fmaxf(sc[2], sc[3]))), -3.38e38f);
  asm volatile("" : "+v"(mw));
  SB();
  static_for<target(63), target(64)>(issue);
  float p[4];
#pragma unroll
  for (int g = 0; g < 4; ++g) p[g] = __builtin_amdgcn_exp2f((sc[g] - mw) * LOG2E);
  const float lsum = wave_sum((p[0] + p[1]) + (p[2] + p[3]));
  const int rec = WS_REC + (b * NH + h) * 256;
  {
    const u32x4 r = {__float_as_uint(mw), tag0, __float_as_uint(lsum), tag1};
    __builtin_amdgcn_raw_buffer_store_b128(r, rws, lane == 0 ? (s * 8 + w) * 16 : OOB, rec, AUX_SC1);
  }
  SB();

  Acc8 acc; acc.zero();
  const bool red = w == 7 && bid < 160;
  const int rb = bid & 3, cg = bid >> 2;
  u32x4 r1;
  u32x2 rt;
  unsigned xo = 0;
  int tlag = 0;
  unsigned ow = 0;
  const bool odd = lane & 1;
  int so_ = (odd && row) ? OOB : (5 * w + (odd ? 4 : row)) * 8192 + (c >> 1) * 16;
  asm volatile("" : "+v"(so_));
  float fw = 0.f;
  static_for<0, 64>([&](auto I) {
    constexpr int st = decltype(I)::value;
    fma8(acc, dpp<BCAST + (st & 15)>(p[st >> 4]), kv[64 + st]);
    acc.pin();
    SB();
    if constexpr (st > 0) static_for<target(63 + st), target(64 + st)>(issue);
    side(IC<64 + st>{});
    if constexpr (st == RLD) { MEMBAR(); r1 = ld16<AUX_SC1>(rws, c * 16, rec); SB(); }
    if constexpr (st == RLD + 2) { xo = __builtin_amdgcn_raw_buffer_load_b16(mk(x + rb * DM, red ? DM * 2 : 0), (cg * 16 + 2 * (lane & 7) + (lane >> 5)) * 2, 0, 0); SB(); }
    if constexpr (st == RLD + 4) { ow = __builtin_amdgcn_raw_buffer_load_b32(mk(out, red ? NB * DM * 2 : 0), (rb * DM + cg * 16) * 2, 0, 16); SB(); }
    if constexpr (st == RLD + 16) asm volatile("" :: "v"(ow));
    if constexpr (st == RLD - 2) { MEMBAR(); rt = __builtin_amdgcn_raw_buffer_load_b64(rws, red ? lane * 8 : OOB, WS_T0 + rb * 512, AUX_SC1); SB(); }
    if constexpr (st == RLD + RING) {
      const int dt = (int)(rt[0] - tbase);
      tlag = (int)wave_max(rt[1] == tag0 && dt < 150 ? (float)dt : -1000.f);
      asm volatile("" : "+v"(tlag));
      SB();
    }
    if constexpr (st == RLD + RING + 2) {
      if (__builtin_expect(__builtin_amdgcn_ballot_w64(((r1[1] ^ tag0) | (r1[3] ^ tag1)) != 0u) != 0ull, 0)) {
        unsigned spin = 0;
        do {
          __builtin_amdgcn_s_sleep(8); MEMBAR();
          r1 = ld16<AUX_SC1>(rws, c * 16, rec);
          __builtin_amdgcn_s_waitcnt(0x0F70);
        } while (__builtin_amdgcn_ballot_w64(((r1[1] ^ tag0) | (r1[3] ^ tag1)) != 0u) != 0ull && ++spin < SPIN_CAP);
      }
      const float mi = __uint_as_float(r1[0]), li = __uint_as_float(r1[2]);
      const float M = row_max(mi);
      const float L = row_sum(li * __builtin_amdgcn_exp2f((mi - M) * LOG2E));
      fw = __builtin_amdgcn_exp2f((mw - M) * LOG2E) / L;
      asm volatile("" : "+v"(fw));
      SB();
    }
  });
  {
    const float e0 = fold32(acc[0], acc[4]), e1 = fold32(acc[1], acc[5]), e2 = fold32(acc[2], acc[6]), e3 = fold32(acc[3], acc[7]);
    ((unsigned*)lds)[L_OP + w * 72 + 4 * c + row] = pk_bf16(fold16(e0, e2) * fw, fold16(e1, e3) * fw);
  }
  __syncthreads();
  {
    u32x4 af[4];
#pragma unroll
    for (int ks = 0; ks < 4; ++ks) af[ks] = *(const u32x4*)(lds + L_OP + (lane & 7) * 72 + ks * 16 + row * 4);
#pragma unroll
    for (int ks = 0; ks < 4; ++ks)
#pragma unroll
      for (int t = 0; t < 5; ++t) y[t] = mfma(af[ks], bfrag[ks][t], y[t]);
    float sm[5];
#pragma unroll
    for (int t = 0; t < 5; ++t) sm[t] = (y[t][0] + y[t][1]) + (y[t][2] + y[t][3]);
    const float v01 = fold16(sm[0], sm[1]), v23 = fold16(sm[2], sm[3]), v4 = fold16(sm[4], sm[4]);
    const float v = lane < 32 ? v01 : v23;
    const float vn = dpp<XOR1>(v), v4n = dpp<XOR1>(v4);
    __builtin_amdgcn_raw_buffer_store_b128(u32x4{__float_as_uint(odd ? v4n : v), tag0, __float_as_uint(odd ? v4 : vn), tag1}, rws,
                                           so_, WS_SLAB + b * 64 * DM * 8 + pidx * 128, AUX_SC1);
  }

  if (red) {
    const int so = WS_SLAB + rb * 64 * DM * 8 + cg * 8192, vo = lane * 16;
    auto poll = [&](u32x4 (&t)[8]) {
      MEMBAR();
#pragma unroll
      for (int i = 0; i < 8; ++i) { t[i] = ld16<AUX_SC1>(rws, vo + (i & 3) * 1024, so + (i >> 2) * 4096); SB(); }
    };
    f32x2 f;
    auto take = [&](u32x4 (&t)[8]) -> bool {
      unsigned bad = 0;
      f = f32x2{0.f, 0.f};
#pragma unroll
      for (int i = 0; i < 8; ++i) {
        bad |= (t[i][1] ^ tag0) | (t[i][3] ^ tag1); f[0] += __uint_as_float(t[i][0]); f[1] += __uint_as_float(t[i][2]);
        asm volatile("" : "+v"(bad), "+v"(f));
      }
      return __builtin_amdgcn_ballot_w64(bad != 0u) == 0ull;
    };
    u32x4 ta[8];
    gate<due_at(126) + PD0>(tbase + (unsigned)__builtin_amdgcn_readfirstlane(tlag));
    for (unsigned spin = 0; spin < SPIN_CAP; ++spin) { poll(ta); if (take(ta)) break; }
    float v = fold32(f[0], f[1]);
    v = fold16(v, v); v += dpp<ROR8>(v);
    __builtin_amdgcn_raw_buffer_store_b16(__builtin_bit_cast(u16, (__bf16)(lo_f(xo) + v)), mk(out, NB * DM * 2),
                                          (lane & 24) == 0 ? (rb * DM + cg * 16 + 2 * (lane & 7) + (lane >> 5)) * 2 : OOB, 0, 16);
    if (lane == 0 && bid == 0) __builtin_amdgcn_raw_buffer_store_b32(tag0, rws, WS_EPOCH, 0, AUX_SC1);
  }
  asm volatile(".Lfd_code1:");
}

}

extern "C" void kernel_launch(void* const* d_in, const int* in_sizes, int n_in, void* d_out, int out_size, void* d_ws, size_t ws_size,
                              hipStream_t stream) {
  (void)in_sizes; (void)n_in; (void)out_size; (void)ws_size;
  fused_decode<<<NWG, NTHR, 0, stream>>>((const u16*)d_in[0], (const u16*)d_in[1], (const u16*)d_in[2], (const u16*)d_in[3],
                                         (const u16*)d_in[4], (const u16*)d_in[6], (const u16*)d_in[7], (const u16*)d_in[8],
                                         (u16*)d_out, (unsigned char*)d_ws);
}
```

```cpp
#include <hip/hip_runtime.h>
#include <hip/hip_bf16.h>
#include <stdint.h>

namespace {

constexpr int NB = 4, DM = 640, NH = 32, DH = 128, FF = 1728;
constexpr int NWG = 256, NTHR = 512;

typedef unsigned short u16;
typedef unsigned u32x4 __attribute__((ext_vector_type(4)));
typedef unsigned u32x2 __attribute__((ext_vector_type(2)));
typedef float f32x4 __attribute__((ext_vector_type(4)));
typedef float f32x2 __attribute__((ext_vector_type(2)));
typedef __bf16 bf16x2 __attribute__((ext_vector_type(2)));
typedef short s16x4 __attribute__((ext_vector_type(4)));
typedef __bf16 bf16x8 __attribute__((ext_vector_type(8)));
typedef __amdgpu_buffer_rsrc_t rsrc_t;

constexpr int AUX_NT = 2, AUX_SC1 = 16, RSRC_W3 = 0x00020000;

constexpr int WS_EPOCH = 0, WS_T0 = 2048, WS_REC = 4096, WS_SLAB = WS_REC + NB * NH * 16 * 16, WS_X = WS_SLAB + NB * 64 * DM * 8, WS_END = WS_X + NH * NB * 8192;
constexpr unsigned SPIN_CAP = 1u << 18;
constexpr int OOB = 1 << 28;

constexpr int L_XN = 0;
constexpr int L_PP = L_XN + 640 * 4;
constexpr int L_QP = L_PP + 8 * 256;
constexpr int L_HH = L_QP + 8 * 128;
constexpr int L_OP = L_HH + 8 * 16;
constexpr int L_ST = L_OP + 8 * 72;
constexpr int STP = 328;
constexpr int L_END = L_ST + 2 * 32 * STP;

__device__ __forceinline__ rsrc_t mk(const void* p, unsigned bytes) {
  return __builtin_amdgcn_make_buffer_rsrc(const_cast<void*>(p), 0, (int)bytes, RSRC_W3);
}
template <int AUX> __device__ __forceinline__ u32x4 ld16(rsrc_t r, int voff, int soff) {
  return __builtin_amdgcn_raw_buffer_load_b128(r, voff, soff, AUX);
}
__device__ __forceinline__ float lo_f(unsigned u) { return __builtin_bit_cast(float, u << 16); }
__device__ __forceinline__ float hi_f(unsigned u) { return __builtin_bit_cast(float, u & 0xffff0000u); }
__device__ __forceinline__ float bfr(float f) { return (float)(__bf16)f; }
__device__ __forceinline__ unsigned pk_bf16(float lo, float hi) {
  bf16x2 v = {(__bf16)lo, (__bf16)hi};
  return __builtin_bit_cast(unsigned, v);
}
__device__ __forceinline__ float dot2(unsigned a, unsigned b, float acc) {
  return __builtin_amdgcn_fdot2_f32_bf16(__builtin_bit_cast(bf16x2, a), __builtin_bit_cast(bf16x2, b), acc, false);
}
template <int CTRL> __device__ __forceinline__ float dpp(float x) {
  return __builtin_bit_cast(float, __builtin_amdgcn_mov_dpp(__builtin_bit_cast(int, x), CTRL, 0xf, 0xf, true));
}
constexpr int XOR1 = 0xB1, XOR2 = 0x4E, XOR7 = 0x141, ROR8 = 0x128, BCAST = 0x150;
__device__ __forceinline__ float fold32(float a, float b) {
  auto s = __builtin_amdgcn_permlane32_swap(__builtin_bit_cast(unsigned, a), __builtin_bit_cast(unsigned, b), false, false);
  return __builtin_bit_cast(float, (unsigned)s[0]) + __builtin_bit_cast(float, (unsigned)s[1]);
}
__device__ __forceinline__ float fold16(float a, float b) {
  auto s = __builtin_amdgcn_permlane16_swap(__builtin_bit_cast(unsigned, a), __builtin_bit_cast(unsigned, b), false, false);
  return __builtin_bit_cast(float, (unsigned)s[0]) + __builtin_bit_cast(float, (unsigned)s[1]);
}
__device__ __forceinline__ float row_sum(float v) {
  v += dpp<XOR1>(v); v += dpp<XOR2>(v); v += dpp<XOR7>(v); v += dpp<ROR8>(v); return v;
}
__device__ __forceinline__ float row_max(float v) {
  v = fmaxf(v, dpp<XOR1>(v)); v = fmaxf(v, dpp<XOR2>(v)); v = fmaxf(v, dpp<XOR7>(v)); v = fmaxf(v, dpp<ROR8>(v)); return v;
}
__device__ __forceinline__ float wave_sum(float v) { v = row_sum(v); v = fold16(v, v); return fold32(v, v); }
__device__ __forceinline__ float wave_max(float v) {
  v = row_max(v);
  auto s = __builtin_amdgcn_permlane16_swap(__builtin_bit_cast(unsigned, v), __builtin_bit_cast(unsigned, v), false, false);
  v = fmaxf(__builtin_bit_cast(float, (unsigned)s[0]), __builtin_bit_cast(float, (unsigned)s[1]));
  auto t = __builtin_amdgcn_permlane32_swap(__builtin_bit_cast(unsigned, v), __builtin_bit_cast(unsigned, v), false, false);
  return fmaxf(__builtin_bit_cast(float, (unsigned)t[0]), __builtin_bit_cast(float, (unsigned)t[1]));
}
struct Acc8 {
  f32x2 v[4];
  __device__ __forceinline__ void zero() { v[0] = v[1] = v[2] = v[3] = f32x2{0.f, 0.f}; }
  __device__ __forceinline__ float operator[](int i) const { return v[i >> 1][i & 1]; }
  __device__ __forceinline__ void pin() { asm volatile("" : "+v"(v[0]), "+v"(v[1]), "+v"(v[2]), "+v"(v[3])); }
};
__device__ __forceinline__ void fma8(Acc8& acc, float s, u32x4 v) {
  const f32x2 ss = {s, s};
#pragma unroll
  for (int k = 0; k < 4; ++k) acc.v[k] = __builtin_elementwise_fma(ss, f32x2{lo_f(v[k]), hi_f(v[k])}, acc.v[k]);
}
#define MEMBAR() asm volatile("" ::: "memory")
#define SB() __builtin_amdgcn_sched_barrier(0)
#define WAVE_SYNC() do { __builtin_amdgcn_fence(__ATOMIC_RELEASE, "wavefront"); __builtin_amdgcn_wave_barrier(); __builtin_amdgcn_fence(__ATOMIC_ACQUIRE, "wavefront"); } while (0)

template <int I> struct IC { static constexpr int value = I; };
template <int I, int N, class F> __device__ __forceinline__ void static_for(F&& f) {
  if constexpr (I < N) { f(IC<I>{}); static_for<I + 1, N>(f); }
}
__device__ __forceinline__ float transpose_reduce(float (&x)[16], int c) {
#define TR_STEP(HALF, CTRL) _Pragma("unroll") for (int i = 0; i < (HALF); ++i) { \
    const float lo_ = x[i], hi_ = x[i + (HALF)]; const float t1_ = lo_ + dpp<CTRL>(lo_), t2_ = hi_ + dpp<CTRL>(hi_); \
    x[i] = (c & (HALF)) ? t2_ : t1_; }
  TR_STEP(8, ROR8) TR_STEP(4, XOR7) TR_STEP(2, XOR2) TR_STEP(1, XOR1)
#undef TR_STEP
  return x[0];
}

constexpr double TAU = 26.75;
constexpr int RING = 8;
constexpr int XP = 15, PRE = 24;
constexpr int D0 = 33, RLD = 40;
constexpr int PD0 = 147;
static_assert(D0 + 3 * 24 + 4 < 124, "wo lands before its use");
constexpr int slot_at(int n, int first, int cnt) { return (n >= first && (n - first) % 3 == 0 && (n - first) / 3 < cnt) ? (n - first) / 3 : -1; }
constexpr bool gated(int n) { return (n >= 2 && n <= 11) || (n > 11 && n <= XP ? (n & 1) != 0 : (n & 1) == 0); }
constexpr int gate_no(int n) { int g = 0; for (int m = 0; m < n; ++m) g += gated(m); return g; }
constexpr int EXTRA[18] = {0, 2, 2, 2, 2, 2, 10, 10, 2, -11, 8, 0, -9, 0, 0, 0, 0, 12};
constexpr int due_at(int n) { double t = 0; for (int g = 0; g < gate_no(n); ++g) t += 2 * TAU + (g < 18 ? EXTRA[g] : g == 41 ? 4 : 0); return (int)t; }
constexpr int target(int u) {
  const int drip = PRE + 2 * ((u + 8) / 8), mid = u >= 64 ? 64 + RING + 2 : 0;
  int t = (u + 1 + RING) & ~1;
  t = t < drip ? drip : t; t = t < mid ? mid : t;
  return u < 0 ? PRE : (t > 128 ? 128 : t);
}
constexpr int RCOL[12] = {33, 62, 8, 41, 48, 9, 40, 28, 32, 56, 4, 0}, OCOL[6] = {33, 52, 1, 16, 15, 19};
constexpr int ord(int n) { int r = 0; for (int k = 0; k < 6; ++k) if ((n >> k) & 1) r ^= OCOL[k]; return r; }
constexpr bool ord_is_bijective() { unsigned long long m = 0; for (int n = 0; n < 64; ++n) m |= 1ull << ord(n); return m == ~0ull; }
static_assert(ord_is_bijective(), "every piece is read once");
constexpr unsigned long long rtab(int k0) { unsigned long long t = 0; for (int i = 0; i < 8; ++i) { int r = 0; for (int k = 0; k < 3; ++k) if ((i >> k) & 1) r ^= RCOL[k0 + k]; t |= (unsigned long long)r << (8 * i); } return t; }
template <int DUE> __device__ __forceinline__ void gate(unsigned base) {
  asm volatile("1:\n\ts_memrealtime vcc\n\ts_waitcnt lgkmcnt(0)\n\ts_sub_u32 vcc_lo, vcc_lo, %0\n\ts_cmp_ge_i32 vcc_lo, %1\n\ts_cbranch_scc1 2f\n\ts_sleep 1\n\ts_branch 1b\n2:"
               :: "s"(base), "n"(DUE) : "vcc", "scc");
}

__global__ void __launch_bounds__(NTHR, 2)
fused_decode(const u16* x, const u16* __restrict__ kc, const u16* __restrict__ vc, const u16* __restrict__ wq,
             const u16* __restrict__ wo, const u16* __restrict__ wg, const u16* __restrict__ wu, const u16* __restrict__ wd,
             u16* __restrict__ out, unsigned char* ws) {
  __shared__ __attribute__((aligned(16))) float lds[L_END];
  SB();
  const int tid = threadIdx.x, lane = tid & 63, w = __builtin_amdgcn_readfirstlane(tid >> 6);
  const int bid = blockIdx.x, j = bid >> 3;
  const int h = (bid & 7) * 4 + (j >> 3), b = (j & 7) >> 1, s = j & 1;
  const int pidx = 2 * h + s;
  const int row = lane >> 4, c = lane & 15;
  const unsigned rid = (unsigned)(((b * NH + h) * 2 + s) * 8 + w);
  const unsigned rot = (unsigned)((rtab(0) >> (8 * (rid & 7))) ^ (rtab(3) >> (8 * ((rid >> 3) & 7))) ^ (rtab(6) >> (8 * ((rid >> 6) & 7))) ^ (rtab(9) >> (8 * (rid >> 9)))) & 63u;
  int voffs[4];
#pragma unroll
  for (int k = 0; k < 4; ++k) voffs[k] = lane * 16 + (int)((k ^ rot) & 3u) * 1024;
  const int rhi = (int)(rot >> 2) * 4096;
  u32x4 kv[128], wb[5], wst[25], rx4;
  unsigned tbase;
  {
    const unsigned ub = bid, pord = (ub & 0x2Cu) | ((ub & 0x41u) << 1) | ((ub & 2u) << 5) | ((ub >> 4) & 1u) | ((ub >> 3) & 16u);
    const unsigned phi = (pord * (unsigned)(4 * TAU)) >> 9;
    const int r1_ = rhi ^ ((ord(1) >> 2) * 4096);
    asm volatile("" :: "s"(phi), "s"(rhi), "s"(r1_), "v"(voffs[0]), "v"(voffs[ord(1) & 3]));
    SB();
    asm volatile("s_memrealtime vcc\n\ts_waitcnt lgkmcnt(0)\n\ts_add_u32 %0, vcc_lo, %1" : "=s"(tbase) : "s"(phi), "s"(kc), "s"(wd), "s"(ws), "s"(rhi), "s"(r1_), "v"(voffs[0]), "v"(voffs[ord(1) & 3]) : "vcc", "scc");
  }
  const rsrc_t rk = mk(kc + (size_t)rid * 32768, 65536);
  gate<0>(tbase);
  kv[0] = ld16<AUX_NT>(rk, voffs[0], rhi); kv[1] = ld16<AUX_NT>(rk, voffs[ord(1) & 3], rhi ^ ((ord(1) >> 2) * 4096));
  asm volatile("" : "+s"(x), "+s"(vc), "+s"(wq), "+s"(wo), "+s"(wg), "+s"(wu), "+s"(wd), "+s"(ws));
  SB();
  unsigned xr[5];
  {
    const rsrc_t rxw = mk(x + (w & 3) * DM, w < 4 ? DM * 2 : 0);
#pragma unroll
    for (int i = 0; i < 5; ++i) xr[i] = __builtin_amdgcn_raw_buffer_load_b32(rxw, lane * 4 + i * 256, 0, 0);
  }
  unsigned cw[3];
  {
    unsigned long long pc; unsigned len;
    asm volatile("s_getpc_b64 %0\n.Lfd_code0:\n\ts_mov_b32 %1, .Lfd_code1-.Lfd_code0" : "=s"(pc), "=s"(len));
    const rsrc_t rc = mk((const void*)pc, (j & 15) == 0 ? len : 0u);
#pragma unroll
    for (int i = 0; i < 3; ++i) cw[i] = __builtin_amdgcn_raw_buffer_load_b32(rc, w == 5 ? lane * 128 : OOB, i * 8192, 0);
  }
  const rsrc_t rws = mk(ws, WS_END);
  const unsigned epoch = __builtin_amdgcn_raw_buffer_load_b32(rws, WS_EPOCH, 0, AUX_SC1);
  MEMBAR();
  SB();
  const rsrc_t rv = mk(vc + (size_t)rid * 32768, 65536);
  const int fb = (j >> 3) * 8 + (bid & 7);
  const bool ffn = fb < FF / 64;
  const int pc = ffn ? fb : 0;
  const rsrc_t ro = mk(wo + (size_t)h * DH * DM, DH * DM * 2);
  const rsrc_t rd = mk(wd + (size_t)(2 * pc + s) * 32 * DM, ffn ? 32 * DM * 2 : 0);
  const int xoff = WS_X + ((h * NB + b) * 8 + w) * 1024;
  const bool xuse = ffn || w < 4;
  const int krow = 160 * b + 40 * (w & 3) + (lane >> 3);
  const int pitch = w < 4 ? NH * DH * 2 : FF * 2, col = w < 4 ? h * DH + s * 64 : pc * 64;
  const rsrc_t rw = mk((w < 4 ? wq : s ? wu : wg) + col, xuse ? (unsigned)(DM * pitch - col * 2) : 0u);
  const bool turn = w >= 4 && s;
  const int vw = krow * pitch + (lane & 7) * 16;
  unsigned lw[3];
  auto issue = [&](auto I) {
    constexpr int n = decltype(I)::value;
    if constexpr (gated(n)) gate<due_at(n)>(tbase);
    constexpr int m = ord(n & 63);
    kv[n] = ld16<AUX_NT>(n < 64 ? rk : rv, voffs[m & 3], rhi ^ ((m >> 2) * 4096));
    SB();
    constexpr int id = slot_at(n, D0, 25);
    if constexpr (n >= 2 && n < 7) wb[n - 2] = ld16<AUX_NT>(rw, vw, (turn ? (n - 2 + 2) % 5 : n - 2) * 8 * pitch);
    if constexpr (n == 7 || n == 8) lw[n - 7] = __builtin_amdgcn_raw_buffer_load_b32(ro, lane < 10 ? lane * 128 : OOB, (((j & 7) * 8 + w) * 2 + n - 7) * 1280, 0);
    if constexpr (n == 9) lw[2] = __builtin_amdgcn_raw_buffer_load_b32(rd, lane < 10 ? lane * 128 : OOB, (b * 8 + w) * 1280, 0);
    if constexpr (n == XP) {
      MEMBAR();
      rx4 = ld16<AUX_SC1>(rws, xuse ? lane * 16 : OOB, xoff);
    }
    if constexpr (id >= 0) wst[id] = ld16<0>(id < 5 ? rd : ro, tid * 16, (id < 5 ? id : id - 5) * 8192);
    SB();
  };
  static_for<2, 4>(issue);
  if (w < 4) {
    float xv[10], sum = 0.f;
#pragma unroll
    for (int i = 0; i < 5; ++i) { xv[2 * i] = lo_f(xr[i]); xv[2 * i + 1] = hi_f(xr[i]); sum += xv[2 * i] + xv[2 * i + 1]; }
    const float mean = wave_sum(sum) * (1.f / DM);
    float sq = 0.f;
#pragma unroll
    for (int i = 0; i < 10; ++i) { const float d = xv[i] - mean; sq = fmaf(d, d, sq); }
    const float var = bfr(wave_sum(sq) * (1.f / DM));
    const float mb = bfr(mean), rs = 1.f / bfr(sqrtf(bfr(var + 1e-5f)));
#pragma unroll
    for (int i = 0; i < 10; ++i) lds[L_XN + (2 * lane + 128 * (i >> 1) + (i & 1)) * 4 + w] = bfr(bfr(xv[i] - mb) * rs);
  }
  SB();
  static_for<4, 6>(issue);
  __syncthreads();
  asm volatile("" :: "v"(cw[0]), "v"(cw[1]), "v"(cw[2]));
  const unsigned tag0 = epoch + 1u, tag1 = (epoch + 1u) * 0x9E3779B1u + 0x7F4A7C15u;
  {
    Acc8 a[4];
#pragma unroll
    for (int bb = 0; bb < 4; ++bb) a[bb].zero();
    static_for<0, 5>([&](auto T) {
      constexpr int t = decltype(T)::value;
      const f32x4 xn4 = *(const f32x4*)(lds + L_XN + (krow + 8 * (turn ? (t + 2) % 5 : t)) * 4);
#pragma unroll
      for (int bb = 0; bb < 4; ++bb) { fma8(a[bb], xn4[bb], wb[t]); a[bb].pin(); }
      SB();
      if constexpr (t >= 1 && t < 4) issue(IC<5 + t>{});
    });
    float v[8];
#pragma unroll
    for (int k = 0; k < 8; ++k) { v[k] = fold16(fold32(a[0][k], a[2][k]), fold32(a[1][k], a[3][k])); v[k] += dpp<ROR8>(v[k]); }
    const bool up = lane & 8;
    *(f32x4*)(lds + L_PP + w * 256 + row * 64 + (lane & 7) * 8 + (up ? 4 : 0)) = f32x4{up ? v[4] : v[0], up ? v[5] : v[1], up ? v[6] : v[2], up ? v[7] : v[3]};
  }
  SB();
  issue(IC<9>{});
  __syncthreads();
  if (w < 4) {
    f32x2 v = {0.f, 0.f};
#pragma unroll
    for (int i = 0; i < 4; ++i) v += *(const f32x2*)(lds + L_PP + ((tid >> 7) * 4 + i) * 256 + 2 * (tid & 127));
    const u32x4 r = {__float_as_uint(v[0]), tag0, __float_as_uint(v[1]), tag1};
    const int o = (((tid >> 5) & 3) * 8 + (tid >> 7) * 4 + b) * 1024 + s * 512 + (tid & 31) * 16;
    __builtin_amdgcn_raw_buffer_store_b128(r, rws, (ffn || w < 2) ? o : OOB, WS_X + h * NB * 8192, AUX_SC1);
  } else if (w == 7) {
    __builtin_amdgcn_raw_buffer_store_b64(u32x2{tbase, tag0}, rws, lane == 0 ? (b * 64 + pidx) * 8 : OOB, WS_T0, AUX_SC1);
  }
  SB();
  static_for<10, PRE - 2>(issue);
  asm volatile("" :: "v"(lw[0]), "v"(lw[1]), "v"(lw[2]));
  if (__builtin_expect(xuse && __builtin_amdgcn_ballot_w64(((rx4[1] ^ tag0) | (rx4[3] ^ tag1)) != 0u) != 0ull, 0)) {
    unsigned spin = 0;
    do {
      MEMBAR();
      rx4 = ld16<AUX_SC1>(rws, lane * 16, xoff);
      __builtin_amdgcn_s_waitcnt(0x0F70);
    } while (__builtin_amdgcn_ballot_w64(((rx4[1] ^ tag0) | (rx4[3] ^ tag1)) != 0u) != 0ull && ++spin < SPIN_CAP);
  }
  *(f32x2*)(lds + L_QP + w * 128 + 2 * lane) = f32x2{__uint_as_float(rx4[0]), __uint_as_float(rx4[2])};
  SB();
  static_for<PRE - 2, PRE>(issue);
  __syncthreads();
  u32x4 q;
  {
    f32x4 s0 = {0.f, 0.f, 0.f, 0.f}, s1 = {0.f, 0.f, 0.f, 0.f};
    float gu = 0.f;
#pragma unroll
    for (int kq = 0; kq < 4; ++kq) {
      s0 += *(const f32x4*)(lds + L_QP + kq * 128 + 8 * c); s1 += *(const f32x4*)(lds + L_QP + kq * 128 + 8 * c + 4);
      gu += lds[L_QP + (4 + kq) * 128 + (lane >> 5) * 64 + 32 * s + (lane & 31)];
    }
    q[0] = pk_bf16(s0[0], s0[1]); q[1] = pk_bf16(s0[2], s0[3]); q[2] = pk_bf16(s1[0], s1[1]); q[3] = pk_bf16(s1[2], s1[3]);
    gu = bfr(gu);
    auto sw = __builtin_amdgcn_permlane32_swap(__float_as_uint(gu), __float_as_uint(gu), false, false);
    const float hv = bfr(__uint_as_float(sw[0]) * __uint_as_float(sw[1]));
    if (lane < 32) ((u16*)(lds + L_HH + w * 16))[lane] = (u16)(__float_as_uint(hv) >> 16);
    WAVE_SYNC();
  }

  f32x4 y[5];
  u32x4 bfrag[4][5];
  auto mfma = [](u32x4 a, u32x4 bb, f32x4 cacc) { return __builtin_amdgcn_mfma_f32_16x16x32_bf16(__builtin_bit_cast(bf16x8, a), __builtin_bit_cast(bf16x8, bb), cacc, 0, 0, 0); };
  auto side = [&](auto U) {
    constexpr int u = decltype(U)::value;
    constexpr int iw = slot_at(u - 1, D0, 25), ir = slot_at(u - 2, D0, 25);
    if constexpr (iw >= 0) {
      const int qq = (iw % 5) * 512 + tid;
      *(u32x4*)(lds + L_ST + ((iw / 5) & 1) * 32 * STP + (qq / 80) * STP + (qq % 80) * 4) = wst[iw];
      if constexpr (iw % 5 == 4) __syncthreads();
      SB();
    }
    if constexpr (ir >= 0 && ir % 5 == 4) {
      const __attribute__((address_space(3))) char* tp = (const __attribute__((address_space(3))) char*)(lds + L_ST + ((ir / 5) & 1) * 32 * STP) + (8 * row + (c >> 2)) * (STP * 4) + w * 160 + (c & 3) * 8;
      u32x4 f[5];
#pragma unroll
      for (int t = 0; t < 5; ++t) {
        const u32x2 lo = __builtin_bit_cast(u32x2, __builtin_amdgcn_ds_read_tr16_b64_v4i16((__attribute__((address_space(3))) s16x4*)(tp + t * 32)));
        const u32x2 hi = __builtin_bit_cast(u32x2, __builtin_amdgcn_ds_read_tr16_b64_v4i16((__attribute__((address_space(3))) s16x4*)(tp + 4 * STP * 4 + t * 32)));
        f[t] = u32x4{lo[0], lo[1], hi[0], hi[1]};
      }
      if constexpr (ir == 4) {
        u32x4 ha = *(const u32x4*)(lds + L_HH + w * 16 + row * 4);
        if (lane & 7) ha = u32x4{0u, 0u, 0u, 0u};
#pragma unroll
        for (int t = 0; t < 5; ++t) { y[t] = mfma(ha, f[t], f32x4{0.f, 0.f, 0.f, 0.f}); asm volatile("" : "+v"(y[t])); }
      } else {
#pragma unroll
        for (int t = 0; t < 5; ++t) { bfrag[ir / 5 - 1][t] = f[t]; asm volatile("" : "+v"(bfrag[ir / 5 - 1][t])); }
      }
      SB();
    }
  };

  constexpr float SCALE = 0.08838834764831845f, LOG2E = 1.4426950408889634f;
  float sc[4];
  {
    float xx[16];
    static_for<0, 64>([&](auto I) {
      constexpr int st = decltype(I)::value;
      float d = dot2(kv[st][0], q[0], 0.f);
      d = dot2(kv[st][1], q[1], d); d = dot2(kv[st][2], q[2], d); d = dot2(kv[st][3], q[3], d);
      asm volatile("" : "+v"(d));
      xx[st & 15] = d;
      SB();
      static_for<target(st - 1), target(st)>(issue);
      side(I);
      if constexpr ((st & 15) == 15) { sc[st >> 4] = bfr(bfr(transpose_reduce(xx, c)) * SCALE); SB(); }
    });
  }
  float mw = fmaxf(wave_max(fmaxf(fmaxf(sc[0], sc[1]), fmaxf(sc[2], sc[3]))), -3.38e38f);
  asm volatile("" : "+v"(mw));
  SB();
  static_for<target(63), target(64)>(issue);
  float p[4];
#pragma unroll
  for (int g = 0; g < 4; ++g) p[g] = __builtin_amdgcn_exp2f((sc[g] - mw) * LOG2E);
  const float lsum = wave_sum((p[0] + p[1]) + (p[2] + p[3]));
  const int rec = WS_REC + (b * NH + h) * 256;
  {
    const u32x4 r = {__float_as_uint(mw), tag0, __float_as_uint(lsum), tag1};
    __builtin_amdgcn_raw_buffer_store_b128(r, rws, lane == 0 ? (s * 8 + w) * 16 : OOB, rec, AUX_SC1);
  }
  SB();

  Acc8 acc; acc.zero();
  const bool red = w == 7 && bid < 160;
  const int rb = bid & 3, cg = bid >> 2;
  u32x4 r1;
  u32x2 rt;
  unsigned xo = 0;
  int tlag = 0;
  unsigned ow = 0;
  const bool odd = lane & 1;
  int so_ = (odd && row) ? OOB : (5 * w + (odd ? 4 : row)) * 8192 + (c >> 1) * 16;
  asm volatile("" : "+v"(so_));
  float fw = 0.f;
  static_for<0, 64>([&](auto I) {
    constexpr int st = decltype(I)::value;
    fma8(acc, dpp<BCAST + (st & 15)>(p[st >> 4]), kv[64 + st]);
    acc.pin();
    SB();
    if constexpr (st > 0) static_for<target(63 + st), target(64 + st)>(issue);
    side(IC<64 + st>{});
    if constexpr (st == RLD) { MEMBAR(); r1 = ld16<AUX_SC1>(rws, c * 16, rec); SB(); }
    if constexpr (st == RLD + 2) { xo = __builtin_amdgcn_raw_buffer_load_b16(mk(x + rb * DM, red ? DM * 2 : 0), (cg * 16 + 2 * (lane & 7) + (lane >> 5)) * 2, 0, 0); SB(); }
    if constexpr (st == RLD + 4) { ow = __builtin_amdgcn_raw_buffer_load_b32(mk(out, red ? NB * DM * 2 : 0), (rb * DM + cg * 16) * 2, 0, AUX_SC1); SB(); }
    if constexpr (st == RLD + 16) asm volatile("" :: "v"(ow));
    if constexpr (st == RLD - 2) { MEMBAR(); rt = __builtin_amdgcn_raw_buffer_load_b64(rws, red ? lane * 8 : OOB, WS_T0 + rb * 512, AUX_SC1); SB(); }
    if constexpr (st == RLD + RING) {
      const int dt = (int)(rt[0] - tbase);
      tlag = (int)wave_max(rt[1] == tag0 && dt < 150 ? (float)dt : -1000.f);
      asm volatile("" : "+v"(tlag));
      SB();
    }
    if constexpr (st == RLD + RING + 2) {
      if (__builtin_expect(__builtin_amdgcn_ballot_w64(((r1[1] ^ tag0) | (r1[3] ^ tag1)) != 0u) != 0ull, 0)) {
        unsigned spin = 0;
        do {
          __builtin_amdgcn_s_sleep(8); MEMBAR();
          r1 = ld16<AUX_SC1>(rws, c * 16, rec);
          __builtin_amdgcn_s_waitcnt(0x0F70);
        } while (__builtin_amdgcn_ballot_w64(((r1[1] ^ tag0) | (r1[3] ^ tag1)) != 0u) != 0ull && ++spin < SPIN_CAP);
      }
      const float mi = __uint_as_float(r1[0]), li = __uint_as_float(r1[2]);
      const float M = row_max(mi);
      const float L = row_sum(li * __builtin_amdgcn_exp2f((mi - M) * LOG2E));
      fw = __builtin_amdgcn_exp2f((mw - M) * LOG2E) / L;
      asm volatile("" : "+v"(fw));
      SB();
    }
  });
  {
    const float e0 = fold32(acc[0], acc[4]), e1 = fold32(acc[1], acc[5]), e2 = fold32(acc[2], acc[6]), e3 = fold32(acc[3], acc[7]);
    ((unsigned*)lds)[L_OP + w * 72 + 4 * c + row] = pk_bf16(fold16(e0, e2) * fw, fold16(e1, e3) * fw);
  }
  __syncthreads();
  {
    u32x4 af[4];
#pragma unroll
    for (int ks = 0; ks < 4; ++ks) af[ks] = *(const u32x4*)(lds + L_OP + (lane & 7) * 72 + ks * 16 + row * 4);
#pragma unroll
    for (int ks = 0; ks < 4; ++ks)
#pragma unroll
      for (int t = 0; t < 5; ++t) y[t] = mfma(af[ks], bfrag[ks][t], y[t]);
    float sm[5];
#pragma unroll
    for (int t = 0; t < 5; ++t) sm[t] = (y[t][0] + y[t][1]) + (y[t][2] + y[t][3]);
    const float v01 = fold16(sm[0], sm[1]), v23 = fold16(sm[2], sm[3]), v4 = fold16(sm[4], sm[4]);
    const float v = lane < 32 ? v01 : v23;
    const float vn = dpp<XOR1>(v), v4n = dpp<XOR1>(v4);
    __builtin_amdgcn_raw_buffer_store_b128(u32x4{__float_as_uint(odd ? v4n : v), tag0, __float_as_uint(odd ? v4 : vn), tag1}, rws,
                                           so_, WS_SLAB + b * 64 * DM * 8 + pidx * 128, AUX_SC1);
  }

  if (red) {
    const int so = WS_SLAB + rb * 64 * DM * 8 + cg * 8192, vo = lane * 16;
    auto poll = [&](u32x4 (&t)[8]) {
      MEMBAR();
#pragma unroll
      for (int i = 0; i < 8; ++i) { t[i] = ld16<AUX_SC1>(rws, vo + (i & 3) * 1024, so + (i >> 2) * 4096); SB(); }
    };
    f32x2 f;
    auto take = [&](u32x4 (&t)[8]) -> bool {
      unsigned bad = 0;
      f = f32x2{0.f, 0.f};
#pragma unroll
      for (int i = 0; i < 8; ++i) {
        bad |= (t[i][1] ^ tag0) | (t[i][3] ^ tag1); f[0] += __uint_as_float(t[i][0]); f[1] += __uint_as_float(t[i][2]);
        asm volatile("" : "+v"(bad), "+v"(f));
      }
      return __builtin_amdgcn_ballot_w64(bad != 0u) == 0ull;
    };
    u32x4 ta[8];
    gate<due_at(126) + PD0>(tbase + (unsigned)__builtin_amdgcn_readfirstlane(tlag));
    for (unsigned spin = 0; spin < SPIN_CAP; ++spin) { poll(ta); if (take(ta)) break; }
    float v = fold32(f[0], f[1]);
    v = fold16(v, v); v += dpp<ROR8>(v);
    __builtin_amdgcn_raw_buffer_store_b16(__builtin_bit_cast(u16, (__bf16)(lo_f(xo) + v)), mk(out, NB * DM * 2),
                                          (lane & 24) == 0 ? (rb * DM + cg * 16 + 2 * (lane & 7) + (lane >> 5)) * 2 : OOB, 0, AUX_SC1);
    if (lane == 0 && bid == 0) __builtin_amdgcn_raw_buffer_store_b32(tag0, rws, WS_EPOCH, 0, AUX_SC1);
  }
  asm volatile(".Lfd_code1:");
}

}

extern "C" void kernel_launch(void* const* d_in, const int* in_sizes, int n_in, void* d_out, int out_size, void* d_ws, size_t ws_size,
                              hipStream_t stream) {
  (void)in_sizes; (void)n_in; (void)out_size; (void)ws_size;
  fused_decode<<<NWG, NTHR, 0, stream>>>((const u16*)d_in[0], (const u16*)d_in[1], (const u16*)d_in[2], (const u16*)d_in[3],
                                         (const u16*)d_in[4], (const u16*)d_in[6], (const u16*)d_in[7], (const u16*)d_in[8],
                                         (u16*)d_out, (unsigned char*)d_ws);
}
```

```cpp
#include <hip/hip_runtime.h>
#include <hip/hip_bf16.h>
#include <stdint.h>

namespace {

constexpr int NB = 4, DM = 640, NH = 32, DH = 128, FF = 1728;
constexpr int NWG = 256, NTHR = 512;

typedef unsigned short u16;
typedef unsigned u32x4 __attribute__((ext_vector_type(4)));
typedef unsigned u32x2 __attribute__((ext_vector_type(2)));
typedef float f32x4 __attribute__((ext_vector_type(4)));
typedef float f32x2 __attribute__((ext_vector_type(2)));
typedef __bf16 bf16x2 __attribute__((ext_vector_type(2)));
typedef short s16x4 __attribute__((ext_vector_type(4)));
typedef __bf16 bf16x8 __attribute__((ext_vector_type(8)));
typedef __amdgpu_buffer_rsrc_t rsrc_t;

constexpr int AUX_NT = 2, AUX_SC1 = 16, RSRC_W3 = 0x00020000;

constexpr int WS_EPOCH = 0, WS_T0 = 2048, WS_REC = 4096, WS_SLAB = WS_REC + NB * NH * 16 * 16, WS_X = WS_SLAB + NB * 64 * DM * 8, WS_END = WS_X + NH * NB * 8192;
constexpr unsigned SPIN_CAP = 1u << 18;
constexpr int OOB = 1 << 28;

constexpr int L_XN = 0;
constexpr int L_PP = L_XN + 640 * 4;
constexpr int L_QP = L_PP + 8 * 256;
constexpr int L_HH = L_QP + 8 * 128;
constexpr int L_OP = L_HH + 8 * 16;
constexpr int L_ST = L_OP + 8 * 72;
constexpr int STP = 328;
constexpr int L_END = L_ST + 2 * 32 * STP;

__device__ __forceinline__ rsrc_t mk(const void* p, unsigned bytes) {
  return __builtin_amdgcn_make_buffer_rsrc(const_cast<void*>(p), 0, (int)bytes, RSRC_W3);
}
template <int AUX> __device__ __forceinline__ u32x4 ld16(rsrc_t r, int voff, int soff) {
  return __builtin_amdgcn_raw_buffer_load_b128(r, voff, soff, AUX);
}
__device__ __forceinline__ float lo_f(unsigned u) { return __builtin_bit_cast(float, u << 16); }
__device__ __forceinline__ float hi_f(unsigned u) { return __builtin_bit_cast(float, u & 0xffff0000u); }
__device__ __forceinline__ float bfr(float f) { return (float)(__bf16)f; }
__device__ __forceinline__ unsigned pk_bf16(float lo, float hi) {
  bf16x2 v = {(__bf16)lo, (__bf16)hi};
  return __builtin_bit_cast(unsigned, v);
}
__device__ __forceinline__ float dot2(unsigned a, unsigned b, float acc) {
  return __builtin_amdgcn_fdot2_f32_bf16(__builtin_bit_cast(bf16x2, a), __builtin_bit_cast(bf16x2, b), acc, false);
}
template <int CTRL> __device__ __forceinline__ float dpp(float x) {
  return __builtin_bit_cast(float, __builtin_amdgcn_mov_dpp(__builtin_bit_cast(int, x), CTRL, 0xf, 0xf, true));
}
constexpr int XOR1 = 0xB1, XOR2 = 0x4E, XOR7 = 0x141, ROR8 = 0x128, BCAST = 0x150;
__device__ __forceinline__ float fold32(float a, float b) {
  auto s = __builtin_amdgcn_permlane32_swap(__builtin_bit_cast(unsigned, a), __builtin_bit_cast(unsigned, b), false, false);
  return __builtin_bit_cast(float, (unsigned)s[0]) + __builtin_bit_cast(float, (unsigned)s[1]);
}
__device__ __forceinline__ float fold16(float a, float b) {
  auto s = __builtin_amdgcn_permlane16_swap(__builtin_bit_cast(unsigned, a), __builtin_bit_cast(unsigned, b), false, false);
  return __builtin_bit_cast(float, (unsigned)s[0]) + __builtin_bit_cast(float, (unsigned)s[1]);
}
__device__ __forceinline__ float row_sum(float v) {
  v += dpp<XOR1>(v); v += dpp<XOR2>(v); v += dpp<XOR7>(v); v += dpp<ROR8>(v); return v;
}
__device__ __forceinline__ float row_max(float v) {
  v = fmaxf(v, dpp<XOR1>(v)); v = fmaxf(v, dpp<XOR2>(v)); v = fmaxf(v, dpp<XOR7>(v)); v = fmaxf(v, dpp<ROR8>(v)); return v;
}
__device__ __forceinline__ float wave_sum(float v) { v = row_sum(v); v = fold16(v, v); return fold32(v, v); }
__device__ __forceinline__ float wave_max(float v) {
  v = row_max(v);
  auto s = __builtin_amdgcn_permlane16_swap(__builtin_bit_cast(unsigned, v), __builtin_bit_cast(unsigned, v), false, false);
  v = fmaxf(__builtin_bit_cast(float, (unsigned)s[0]), __builtin_bit_cast(float, (unsigned)s[1]));
  auto t = __builtin_amdgcn_permlane32_swap(__builtin_bit_cast(unsigned, v), __builtin_bit_cast(unsigned, v), false, false);
  return fmaxf(__builtin_bit_cast(float, (unsigned)t[0]), __builtin_bit_cast(float, (unsigned)t[1]));
}
struct Acc8 {
  f32x2 v[4];
  __device__ __forceinline__ void zero() { v[0] = v[1] = v[2] = v[3] = f32x2{0.f, 0.f}; }
  __device__ __forceinline__ float operator[](int i) const { return v[i >> 1][i & 1]; }
  __device__ __forceinline__ void pin() { asm volatile("" : "+v"(v[0]), "+v"(v[1]), "+v"(v[2]), "+v"(v[3])); }
};
__device__ __forceinline__ void fma8(Acc8& acc, float s, u32x4 v) {
  const f32x2 ss = {s, s};
#pragma unroll
  for (int k = 0; k < 4; ++k) acc.v[k] = __builtin_elementwise_fma(ss, f32x2{lo_f(v[k]), hi_f(v[k])}, acc.v[k]);
}
#define MEMBAR() asm volatile("" ::: "memory")
#define SB() __builtin_amdgcn_sched_barrier(0)
#define WAVE_SYNC() do { __builtin_amdgcn_fence(__ATOMIC_RELEASE, "wavefront"); __builtin_amdgcn_wave_barrier(); __builtin_amdgcn_fence(__ATOMIC_ACQUIRE, "wavefront"); } while (0)

template <int I> struct IC { static constexpr int value = I; };
template <int I, int N, class F> __device__ __forceinline__ void static_for(F&& f) {
  if constexpr (I < N) { f(IC<I>{}); static_for<I + 1, N>(f); }
}
__device__ __forceinline__ float transpose_reduce(float (&x)[16], int c) {
#define TR_STEP(HALF, CTRL) _Pragma("unroll") for (int i = 0; i < (HALF); ++i) { \
    const float lo_ = x[i], hi_ = x[i + (HALF)]; const float t1_ = lo_ + dpp<CTRL>(lo_), t2_ = hi_ + dpp<CTRL>(hi_); \
    x[i] = (c & (HALF)) ? t2_ : t1_; }
  TR_STEP(8, ROR8) TR_STEP(4, XOR7) TR_STEP(2, XOR2) TR_STEP(1, XOR1)
#undef TR_STEP
  return x[0];
}

constexpr double TAU = 26.95;
constexpr int RING = 8;
constexpr int XP = 15, PRE = 24;
constexpr int D0 = 33, RLD = 40;
constexpr int PD0 = 147;
static_assert(D0 + 3 * 24 + 4 < 124, "wo lands before its use");
constexpr int slot_at(int n, int first, int cnt) { return (n >= first && (n - first) % 3 == 0 && (n - first) / 3 < cnt) ? (n - first) / 3 : -1; }
constexpr bool gated(int n) { return (n >= 2 && n <= 11) || (n > 11 && n <= XP ? (n & 1) != 0 : (n & 1) == 0); }
constexpr int gate_no(int n) { int g = 0; for (int m = 0; m < n; ++m) g += gated(m); return g; }
constexpr int EXTRA[18] = {0, 2, 2, 2, 2, 2, 10, 10, 2, -11, 8, 0, -9, 0, 0, 0, 0, 12};
constexpr int due_at(int n) { double t = 0; for (int g = 0; g < gate_no(n); ++g) t += 2 * TAU + (g < 18 ? EXTRA[g] : g == 41 ? 4 : 0); return (int)t; }
constexpr int target(int u) {
  const int drip = PRE + 2 * ((u + 8) / 8), mid = u >= 64 ? 64 + RING + 2 : 0;
  int t = (u + 1 + RING) & ~1;
  t = t < drip ? drip : t; t = t < mid ? mid : t;
  return u < 0 ? PRE : (t > 128 ? 128 : t);
}
constexpr int RCOL[12] = {33, 62, 8, 41, 48, 9, 40, 28, 32, 56, 4, 0}, OCOL[6] = {33, 52, 1, 16, 15, 19};
constexpr int ord(int n) { int r = 0; for (int k = 0; k < 6; ++k) if ((n >> k) & 1) r ^= OCOL[k]; return r; }
constexpr bool ord_is_bijective() { unsigned long long m = 0; for (int n = 0; n < 64; ++n) m |= 1ull << ord(n); return m == ~0ull; }
static_assert(ord_is_bijective(), "every piece is read once");
constexpr unsigned long long rtab(int k0) { unsigned long long t = 0; for (int i = 0; i < 8; ++i) { int r = 0; for (int k = 0; k < 3; ++k) if ((i >> k) & 1) r ^= RCOL[k0 + k]; t |= (unsigned long long)r << (8 * i); } return t; }
template <int DUE> __device__ __forceinline__ void gate(unsigned base) {
  asm volatile("1:\n\ts_memrealtime vcc\n\ts_waitcnt lgkmcnt(0)\n\ts_sub_u32 vcc_lo, vcc_lo, %0\n\ts_cmp_ge_i32 vcc_lo, %1\n\ts_cbranch_scc1 2f\n\ts_sleep 1\n\ts_branch 1b\n2:"
               :: "s"(base), "n"(DUE) : "vcc", "scc");
}

__global__ void __launch_bounds__(NTHR, 2)
fused_decode(const u16* x, const u16* __restrict__ kc, const u16* __restrict__ vc, const u16* __restrict__ wq,
             const u16* __restrict__ wo, const u16* __restrict__ wg, const u16* __restrict__ wu, const u16* __restrict__ wd,
             u16* __restrict__ out, unsigned char* ws) {
  __shared__ __attribute__((aligned(16))) float lds[L_END];
  SB();
  const int tid = threadIdx.x, lane = tid & 63, w = __builtin_amdgcn_readfirstlane(tid >> 6);
  const int bid = blockIdx.x, j = bid >> 3;
  const int h = (bid & 7) * 4 + (j >> 3), b = (j & 7) >> 1, s = j & 1;
  const int pidx = 2 * h + s;
  const int row = lane >> 4, c = lane & 15;
  const unsigned rid = (unsigned)(((b * NH + h) * 2 + s) * 8 + w);
  const unsigned rot = (unsigned)((rtab(0) >> (8 * (rid & 7))) ^ (rtab(3) >> (8 * ((rid >> 3) & 7))) ^ (rtab(6) >> (8 * ((rid >> 6) & 7))) ^ (rtab(9) >> (8 * (rid >> 9)))) & 63u;
  int voffs[4];
#pragma unroll
  for (int k = 0; k < 4; ++k) voffs[k] = lane * 16 + (int)((k ^ rot) & 3u) * 1024;
  const int rhi = (int)(rot >> 2) * 4096;
  u32x4 kv[128], wb[5], wst[25], rx4;
  unsigned tbase;
  {
    const unsigned ub = bid, pord = (ub & 0x2Cu) | ((ub & 0x41u) << 1) | ((ub & 2u) << 5) | ((ub >> 4) & 1u) | ((ub >> 3) & 16u);
    const unsigned phi = (pord * (unsigned)(4 * TAU)) >> 9;
    const int r1_ = rhi ^ ((ord(1) >> 2) * 4096);
    asm volatile("" :: "s"(phi), "s"(rhi), "s"(r1_), "v"(voffs[0]), "v"(voffs[ord(1) & 3]));
    SB();
    asm volatile("s_memrealtime vcc\n\ts_waitcnt lgkmcnt(0)\n\ts_add_u32 %0, vcc_lo, %1" : "=s"(tbase) : "s"(phi), "s"(kc), "s"(wd), "s"(ws), "s"(rhi), "s"(r1_), "v"(voffs[0]), "v"(voffs[ord(1) & 3]) : "vcc", "scc");
  }
  const rsrc_t rk = mk(kc + (size_t)rid * 32768, 65536);
  gate<0>(tbase);
  kv[0] = ld16<AUX_NT>(rk, voffs[0], rhi); kv[1] = ld16<AUX_NT>(rk, voffs[ord(1) & 3], rhi ^ ((ord(1) >> 2) * 4096));
  asm volatile("" : "+s"(x), "+s"(vc), "+s"(wq), "+s"(wo), "+s"(wg), "+s"(wu), "+s"(wd), "+s"(ws));
  SB();
  unsigned xr[5];
  {
    const rsrc_t rxw = mk(x + (w & 3) * DM, w < 4 ? DM * 2 : 0);
#pragma unroll
    for (int i = 0; i < 5; ++i) xr[i] = __builtin_amdgcn_raw_buffer_load_b32(rxw, lane * 4 + i * 256, 0, 0);
  }
  unsigned cw[3];
  {
    unsigned long long pc; unsigned len;
    asm volatile("s_getpc_b64 %0\n.Lfd_code0:\n\ts_mov_b32 %1, .Lfd_code1-.Lfd_code0" : "=s"(pc), "=s"(len));
    const rsrc_t rc = mk((const void*)pc, (j & 15) == 0 ? len : 0u);
#pragma unroll
    for (int i = 0; i < 3; ++i) cw[i] = __builtin_amdgcn_raw_buffer_load_b32(rc, w == 5 ? lane * 128 : OOB, i * 8192, 0);
  }
  const rsrc_t rws = mk(ws, WS_END);
  const unsigned epoch = __builtin_amdgcn_raw_buffer_load_b32(rws, WS_EPOCH, 0, AUX_SC1);
  MEMBAR();
  SB();
  const rsrc_t rv = mk(vc + (size_t)rid * 32768, 65536);
  const int fb = (j >> 3) * 8 + (bid & 7);
  const bool ffn = fb < FF / 64;
  const int pc = ffn ? fb : 0;
  const rsrc_t ro = mk(wo + (size_t)h * DH * DM, DH * DM * 2);
  const rsrc_t rd = mk(wd + (size_t)(2 * pc + s) * 32 * DM, ffn ? 32 * DM * 2 : 0);
  const int xoff = WS_X + ((h * NB + b) * 8 + w) * 1024;
  const bool xuse = ffn || w < 4;
  const int krow = 160 * b + 40 * (w & 3) + (lane >> 3);
  const int pitch = w < 4 ? NH * DH * 2 : FF * 2, col = w < 4 ? h * DH + s * 64 : pc * 64;
  const rsrc_t rw = mk((w < 4 ? wq : s ? wu : wg) + col, xuse ? (unsigned)(DM * pitch - col * 2) : 0u);
  const bool turn = w >= 4 && s;
  const int vw = krow * pitch + (lane & 7) * 16;
  unsigned lw[4];
  auto issue = [&](auto I) {
    constexpr int n = decltype(I)::value;
    if constexpr (gated(n)) gate<due_at(n)>(tbase);
    constexpr int m = ord(n & 63);
    kv[n] = ld16<AUX_NT>(n < 64 ? rk : rv, voffs[m & 3], rhi ^ ((m >> 2) * 4096));
    SB();
    constexpr int id = slot_at(n, D0, 25);
    if constexpr (n >= 2 && n < 7) wb[n - 2] = ld16<AUX_NT>(rw, vw, (turn ? (n - 2 + 2) % 5 : n - 2) * 8 * pitch);
    if constexpr (n == 7 || n == 8) lw[n - 7] = __builtin_amdgcn_raw_buffer_load_b32(ro, lane < 10 ? lane * 128 : OOB, (((j & 7) * 8 + w) * 2 + n - 7) * 1280, 0);
    if constexpr (n == 9) lw[2] = __builtin_amdgcn_raw_buffer_load_b32(rd, lane < 10 ? lane * 128 : OOB, (b * 8 + w) * 1280, 0);
    if constexpr (n == 2) {
      const void* tp = (w & 3) == 0 ? (const void*)(vc + (size_t)rid * 32768) : (w & 3) == 1 ? (const void*)(wo + (size_t)h * DH * DM) : (w & 3) == 2 ? (const void*)(wd + (size_t)(2 * pc + s) * 32 * DM) : (const void*)out;
      lw[3] = __builtin_amdgcn_raw_buffer_load_b32(mk(tp, w >= 4 ? 4 : 0), lane * 4, 0, 0);
    }
    if constexpr (n == XP) {
      MEMBAR();
      rx4 = ld16<AUX_SC1>(rws, xuse ? lane * 16 : OOB, xoff);
    }
    if constexpr (id >= 0) wst[id] = ld16<0>(id < 5 ? rd : ro, tid * 16, (id < 5 ? id : id - 5) * 8192);
    SB();
  };
  static_for<2, 4>(issue);
  if (w < 4) {
    float xv[10], sum = 0.f;
#pragma unroll
    for (int i = 0; i < 5; ++i) { xv[2 * i] = lo_f(xr[i]); xv[2 * i + 1] = hi_f(xr[i]); sum += xv[2 * i] + xv[2 * i + 1]; }
    const float mean = wave_sum(sum) * (1.f / DM);
    float sq = 0.f;
#pragma unroll
    for (int i = 0; i < 10; ++i) { const float d = xv[i] - mean; sq = fmaf(d, d, sq); }
    const float var = bfr(wave_sum(sq) * (1.f / DM));
    const float mb = bfr(mean), rs = 1.f / bfr(sqrtf(bfr(var + 1e-5f)));
#pragma unroll
    for (int i = 0; i < 10; ++i) lds[L_XN + (2 * lane + 128 * (i >> 1) + (i & 1)) * 4 + w] = bfr(bfr(xv[i] - mb) * rs);
  }
  SB();
  static_for<4, 6>(issue);
  __syncthreads();
  asm volatile("" :: "v"(cw[0]), "v"(cw[1]), "v"(cw[2]));
  const unsigned tag0 = epoch + 1u, tag1 = (epoch + 1u) * 0x9E3779B1u + 0x7F4A7C15u;
  {
    Acc8 a[4];
#pragma unroll
    for (int bb = 0; bb < 4; ++bb) a[bb].zero();
    static_for<0, 5>([&](auto T) {
      constexpr int t = decltype(T)::value;
      const f32x4 xn4 = *(const f32x4*)(lds + L_XN + (krow + 8 * (turn ? (t + 2) % 5 : t)) * 4);
#pragma unroll
      for (int bb = 0; bb < 4; ++bb) { fma8(a[bb], xn4[bb], wb[t]); a[bb].pin(); }
      SB();
      if constexpr (t >= 1 && t < 4) issue(IC<5 + t>{});
    });
    float v[8];
#pragma unroll
    for (int k = 0; k < 8; ++k) { v[k] = fold16(fold32(a[0][k], a[2][k]), fold32(a[1][k], a[3][k])); v[k] += dpp<ROR8>(v[k]); }
    const bool up = lane & 8;
    *(f32x4*)(lds + L_PP + w * 256 + row * 64 + (lane & 7) * 8 + (up ? 4 : 0)) = f32x4{up ? v[4] : v[0], up ? v[5] : v[1], up ? v[6] : v[2], up ? v[7] : v[3]};
  }
  SB();
  issue(IC<9>{});
  __syncthreads();
  if (w < 4) {
    f32x2 v = {0.f, 0.f};
#pragma unroll
    for (int i = 0; i < 4; ++i) v += *(const f32x2*)(lds + L_PP + ((tid >> 7) * 4 + i) * 256 + 2 * (tid & 127));
    const u32x4 r = {__float_as_uint(v[0]), tag0, __float_as_uint(v[1]), tag1};
    const int o = (((tid >> 5) & 3) * 8 + (tid >> 7) * 4 + b) * 1024 + s * 512 + (tid & 31) * 16;
    __builtin_amdgcn_raw_buffer_store_b128(r, rws, (ffn || w < 2) ? o : OOB, WS_X + h * NB * 8192, AUX_SC1);
  } else if (w == 7) {
    __builtin_amdgcn_raw_buffer_store_b64(u32x2{tbase, tag0}, rws, lane == 0 ? (b * 64 + pidx) * 8 : OOB, WS_T0, AUX_SC1);
  }
  SB();
  static_for<10, PRE - 2>(issue);
  asm volatile("" :: "v"(lw[0]), "v"(lw[1]), "v"(lw[2]), "v"(lw[3]));
  if (__builtin_expect(xuse && __builtin_amdgcn_ballot_w64(((rx4[1] ^ tag0) | (rx4[3] ^ tag1)) != 0u) != 0ull, 0)) {
    unsigned spin = 0;
    do {
      MEMBAR();
      rx4 = ld16<AUX_SC1>(rws, lane * 16, xoff);
      __builtin_amdgcn_s_waitcnt(0x0F70);
    } while (__builtin_amdgcn_ballot_w64(((rx4[1] ^ tag0) | (rx4[3] ^ tag1)) != 0u) != 0ull && ++spin < SPIN_CAP);
  }
  *(f32x2*)(lds + L_QP + w * 128 + 2 * lane) = f32x2{__uint_as_float(rx4[0]), __uint_as_float(rx4[2])};
  SB();
  static_for<PRE - 2, PRE>(issue);
  __syncthreads();
  u32x4 q;
  {
    f32x4 s0 = {0.f, 0.f, 0.f, 0.f}, s1 = {0.f, 0.f, 0.f, 0.f};
    float gu = 0.f;
#pragma unroll
    for (int kq = 0; kq < 4; ++kq) {
      s0 += *(const f32x4*)(lds + L_QP + kq * 128 + 8 * c); s1 += *(const f32x4*)(lds + L_QP + kq * 128 + 8 * c + 4);
      gu += lds[L_QP + (4 + kq) * 128 + (lane >> 5) * 64 + 32 * s + (lane & 31)];
    }
    q[0] = pk_bf16(s0[0], s0[1]); q[1] = pk_bf16(s0[2], s0[3]); q[2] = pk_bf16(s1[0], s1[1]); q[3] = pk_bf16(s1[2], s1[3]);
    gu = bfr(gu);
    auto sw = __builtin_amdgcn_permlane32_swap(__float_as_uint(gu), __float_as_uint(gu), false, false);
    const float hv = bfr(__uint_as_float(sw[0]) * __uint_as_float(sw[1]));
    if (lane < 32) ((u16*)(lds + L_HH + w * 16))[lane] = (u16)(__float_as_uint(hv) >> 16);
    WAVE_SYNC();
  }

  f32x4 y[5];
  u32x4 bfrag[4][5];
  auto mfma = [](u32x4 a, u32x4 bb, f32x4 cacc) { return __builtin_amdgcn_mfma_f32_16x16x32_bf16(__builtin_bit_cast(bf16x8, a), __builtin_bit_cast(bf16x8, bb), cacc, 0, 0, 0); };
  auto side = [&](auto U) {
    constexpr int u = decltype(U)::value;
    constexpr int iw = slot_at(u - 1, D0, 25), ir = slot_at(u - 2, D0, 25);
    if constexpr (iw >= 0) {
      const int qq = (iw % 5) * 512 + tid;
      *(u32x4*)(lds + L_ST + ((iw / 5) & 1) * 32 * STP + (qq / 80) * STP + (qq % 80) * 4) = wst[iw];
      if constexpr (iw % 5 == 4) __syncthreads();
      SB();
    }
    if constexpr (ir >= 0 && ir % 5 == 4) {
      const __attribute__((address_space(3))) char* tp = (const __attribute__((address_space(3))) char*)(lds + L_ST + ((ir / 5) & 1) * 32 * STP) + (8 * row + (c >> 2)) * (STP * 4) + w * 160 + (c & 3) * 8;
      u32x4 f[5];
#pragma unroll
      for (int t = 0; t < 5; ++t) {
        const u32x2 lo = __builtin_bit_cast(u32x2, __builtin_amdgcn_ds_read_tr16_b64_v4i16((__attribute__((address_space(3))) s16x4*)(tp + t * 32)));
        const u32x2 hi = __builtin_bit_cast(u32x2, __builtin_amdgcn_ds_read_tr16_b64_v4i16((__attribute__((address_space(3))) s16x4*)(tp + 4 * STP * 4 + t * 32)));
        f[t] = u32x4{lo[0], lo[1], hi[0], hi[1]};
      }
      if constexpr (ir == 4) {
        u32x4 ha = *(const u32x4*)(lds + L_HH + w * 16 + row * 4);
        if (lane & 7) ha = u32x4{0u, 0u, 0u, 0u};
#pragma unroll
        for (int t = 0; t < 5; ++t) { y[t] = mfma(ha, f[t], f32x4{0.f, 0.f, 0.f, 0.f}); asm volatile("" : "+v"(y[t])); }
      } else {
#pragma unroll
        for (int t = 0; t < 5; ++t) { bfrag[ir / 5 - 1][t] = f[t]; asm volatile("" : "+v"(bfrag[ir / 5 - 1][t])); }
      }
      SB();
    }
  };

  constexpr float SCALE = 0.08838834764831845f, LOG2E = 1.4426950408889634f;
  float sc[4];
  {
    float xx[16];
    static_for<0, 64>([&](auto I) {
      constexpr int st = decltype(I)::value;
      float d = dot2(kv[st][0], q[0], 0.f);
      d = dot2(kv[st][1], q[1], d); d = dot2(kv[st][2], q[2], d); d = dot2(kv[st][3], q[3], d);
      asm volatile("" : "+v"(d));
      xx[st & 15] = d;
      SB();
      static_for<target(st - 1), target(st)>(issue);
      side(I);
      if constexpr ((st & 15) == 15) { sc[st >> 4] = bfr(bfr(transpose_reduce(xx, c)) * SCALE); SB(); }
    });
  }
  float mw = fmaxf(wave_max(fmaxf(fmaxf(sc[0], sc[1]), fmaxf(sc[2], sc[3]))), -3.38e38f);
  asm volatile("" : "+v"(mw));
  SB();
  static_for<target(63), target(64)>(issue);
  float p[4];
#pragma unroll
  for (int g = 0; g < 4; ++g) p[g] = __builtin_amdgcn_exp2f((sc[g] - mw) * LOG2E);
  const float lsum = wave_sum((p[0] + p[1]) + (p[2] + p[3]));
  const int rec = WS_REC + (b * NH + h) * 256;
  {
    const u32x4 r = {__float_as_uint(mw), tag0, __float_as_uint(lsum), tag1};
    __builtin_amdgcn_raw_buffer_store_b128(r, rws, lane == 0 ? (s * 8 + w) * 16 : OOB, rec, AUX_SC1);
  }
  SB();

  Acc8 acc; acc.zero();
  const bool red = w == 7 && bid < 160;
  const int rb = bid & 3, cg = bid >> 2;
  u32x4 r1;
  u32x2 rt;
  unsigned xo = 0;
  int tlag = 0;
  unsigned ow = 0;
  const bool odd = lane & 1;
  int so_ = (odd && row) ? OOB : (5 * w + (odd ? 4 : row)) * 8192 + (c >> 1) * 16;
  asm volatile("" : "+v"(so_));
  float fw = 0.f;
  static_for<0, 64>([&](auto I) {
    constexpr int st = decltype(I)::value;
    fma8(acc, dpp<BCAST + (st & 15)>(p[st >> 4]), kv[64 + st]);
    acc.pin();
    SB();
    if constexpr (st > 0) static_for<target(63 + st), target(64 + st)>(issue);
    side(IC<64 + st>{});
    if constexpr (st == RLD) { MEMBAR(); r1 = ld16<AUX_SC1>(rws, c * 16, rec); SB(); }
    if constexpr (st == RLD + 2) { xo = __builtin_amdgcn_raw_buffer_load_b16(mk(x + rb * DM, red ? DM * 2 : 0), (cg * 16 + 2 * (lane & 7) + (lane >> 5)) * 2, 0, 0); SB(); }
    if constexpr (st == RLD + 4) { ow = __builtin_amdgcn_raw_buffer_load_b32(mk(out, red ? NB * DM * 2 : 0), (rb * DM + cg * 16) * 2, 0, AUX_SC1); SB(); }
    if constexpr (st == RLD + 16) asm volatile("" :: "v"(ow));
    if constexpr (st == RLD - 2) { MEMBAR(); rt = __builtin_amdgcn_raw_buffer_load_b64(rws, red ? lane * 8 : OOB, WS_T0 + rb * 512, AUX_SC1); SB(); }
    if constexpr (st == RLD + RING) {
      const int dt = (int)(rt[0] - tbase);
      tlag = (int)wave_max(rt[1] == tag0 && dt < 150 ? (float)dt : -1000.f);
      asm volatile("" : "+v"(tlag));
      SB();
    }
    if constexpr (st == RLD + RING + 2) {
      if (__builtin_expect(__builtin_amdgcn_ballot_w64(((r1[1] ^ tag0) | (r1[3] ^ tag1)) != 0u) != 0ull, 0)) {
        unsigned spin = 0;
        do {
          __builtin_amdgcn_s_sleep(8); MEMBAR();
          r1 = ld16<AUX_SC1>(rws, c * 16, rec);
          __builtin_amdgcn_s_waitcnt(0x0F70);
        } while (__builtin_amdgcn_ballot_w64(((r1[1] ^ tag0) | (r1[3] ^ tag1)) != 0u) != 0ull && ++spin < SPIN_CAP);
      }
      const float mi = __uint_as_float(r1[0]), li = __uint_as_float(r1[2]);
      const float M = row_max(mi);
      const float L = row_sum(li * __builtin_amdgcn_exp2f((mi - M) * LOG2E));
      fw = __builtin_amdgcn_exp2f((mw - M) * LOG2E) / L;
      asm volatile("" : "+v"(fw));
      SB();
    }
  });
  {
    const float e0 = fold32(acc[0], acc[4]), e1 = fold32(acc[1], acc[5]), e2 = fold32(acc[2], acc[6]), e3 = fold32(acc[3], acc[7]);
    ((unsigned*)lds)[L_OP + w * 72 + 4 * c + row] = pk_bf16(fold16(e0, e2) * fw, fold16(e1, e3) * fw);
  }
  __syncthreads();
  {
    u32x4 af[4];
#pragma unroll
    for (int ks = 0; ks < 4; ++ks) af[ks] = *(const u32x4*)(lds + L_OP + (lane & 7) * 72 + ks * 16 + row * 4);
#pragma unroll
    for (int ks = 0; ks < 4; ++ks)
#pragma unroll
      for (int t = 0; t < 5; ++t) y[t] = mfma(af[ks], bfrag[ks][t], y[t]);
    float sm[5];
#pragma unroll
    for (int t = 0; t < 5; ++t) sm[t] = (y[t][0] + y[t][1]) + (y[t][2] + y[t][3]);
    const float v01 = fold16(sm[0], sm[1]), v23 = fold16(sm[2], sm[3]), v4 = fold16(sm[4], sm[4]);
    const float v = lane < 32 ? v01 : v23;
    const float vn = dpp<XOR1>(v), v4n = dpp<XOR1>(v4);
    __builtin_amdgcn_raw_buffer_store_b128(u32x4{__float_as_uint(odd ? v4n : v), tag0, __float_as_uint(odd ? v4 : vn), tag1}, rws,
                                           so_, WS_SLAB + b * 64 * DM * 8 + pidx * 128, AUX_SC1);
  }

  if (red) {
    const int so = WS_SLAB + rb * 64 * DM * 8 + cg * 8192, vo = lane * 16;
    auto poll = [&](u32x4 (&t)[8]) {
      MEMBAR();
#pragma unroll
      for (int i = 0; i < 8; ++i) { t[i] = ld16<AUX_SC1>(rws, vo + (i & 3) * 1024, so + (i >> 2) * 4096); SB(); }
    };
    f32x2 f;
    auto take = [&](u32x4 (&t)[8]) -> bool {
      unsigned bad = 0;
      f = f32x2{0.f, 0.f};
#pragma unroll
      for (int i = 0; i < 8; ++i) {
        bad |= (t[i][1] ^ tag0) | (t[i][3] ^ tag1); f[0] += __uint_as_float(t[i][0]); f[1] += __uint_as_float(t[i][2]);
        asm volatile("" : "+v"(bad), "+v"(f));
      }
      return __builtin_amdgcn_ballot_w64(bad != 0u) == 0ull;
    };
    u32x4 ta[8];
    gate<due_at(126) + PD0>(tbase + (unsigned)__builtin_amdgcn_readfirstlane(tlag));
    for (unsigned spin = 0; spin < SPIN_CAP; ++spin) { poll(ta); if (take(ta)) break; }
    float v = fold32(f[0], f[1]);
    v = fold16(v, v); v += dpp<ROR8>(v);
    __builtin_amdgcn_raw_buffer_store_b16(__builtin_bit_cast(u16, (__bf16)(lo_f(xo) + v)), mk(out, NB * DM * 2),
                                          (lane & 24) == 0 ? (rb * DM + cg * 16 + 2 * (lane & 7) + (lane >> 5)) * 2 : OOB, 0, AUX_SC1);
    if (lane == 0 && bid == 0) __builtin_amdgcn_raw_buffer_store_b32(tag0, rws, WS_EPOCH, 0, AUX_SC1);
  }
  asm volatile(".Lfd_code1:");
}

}

extern "C" void kernel_launch(void* const* d_in, const int* in_sizes, int n_in, void* d_out, int out_size, void* d_ws, size_t ws_size,
                              hipStream_t stream) {
  (void)in_sizes; (void)n_in; (void)out_size; (void)ws_size;
  fused_decode<<<NWG, NTHR, 0, stream>>>((const u16*)d_in[0], (const u16*)d_in[1], (const u16*)d_in[2], (const u16*)d_in[3],
                                         (const u16*)d_in[4], (const u16*)d_in[6], (const u16*)d_in[7], (const u16*)d_in[8],
                                         (u16*)d_out, (unsigned char*)d_ws);
}
```
